```python
import torch

def _softmax(x, axis=-1):
    return torch.softmax(x, dim=axis)
def _gelu(x):
    return torch.nn.functional.gelu(x, approximate="tanh")
def _silu(x):
    return torch.nn.functional.silu(x)
def _sigmoid(x):
    return torch.sigmoid(x)
def _layernorm(x, axis=-1, eps=1e-5):
    m = x.mean(dim=axis, keepdim=True)
    v = x.var(dim=axis, keepdim=True, unbiased=False)
    return (x - m) / torch.sqrt(v + eps)
def _scatter_add(tgt, idx, src, axis=0):
    return tgt.clone().index_add_(axis, idx, src)
def _segment_sum(src, seg, n):
    out = torch.zeros((n,) + src.shape[1:], dtype=src.dtype)
    return out.index_add_(0, seg, src)
def _pad(x, pw):
    flat = tuple(v for pair in reversed(pw) for v in pair)
    return torch.nn.functional.pad(x, flat)

kernel_name = 'parallel_sum__ssm_conv_h3__moe_arch_mod__prompt'

def setup_inputs():

    _w = (lambda a: (lambda t: t.to(torch.bfloat16) if t.is_floating_point() else t)(torch.as_tensor(a)))
    def _A_setup():
        B, S, D = 1, 2048, 576
        ks = 4
        return {
            "q": _w((lambda *s: torch.randn(*s, dtype=torch.bfloat16) * 0.05)(B, S, D)),
            "k": _w((lambda *s: torch.randn(*s, dtype=torch.bfloat16) * 0.8)(B, S, D)),
            "v": _w((lambda *s: torch.randn(*s, dtype=torch.bfloat16) * 0.8)(B, S, D)),
            "shift_k": _w((lambda *s: torch.randn(*s, dtype=torch.bfloat16) * 0.8)(D, ks) * 0.5),
            "ssm_k": _w((lambda *s: torch.randn(*s, dtype=torch.bfloat16) * 0.8)(D, S) * (1.0 / S ** 0.5)),
            "ssm_D": _w((lambda *s: torch.randn(*s, dtype=torch.bfloat16) * 0.8)(D)),
        }
    _A_in = _A_setup()

    def _B_setup():
        B, S, D, F, C = 1, 2048, 576, 4608, 8
        return {
            "x": _w((lambda *s: torch.randn(*s, dtype=torch.bfloat16) * 0.05)(B, S, D)),
            "w_r": _w((lambda *s: torch.randn(*s, dtype=torch.bfloat16) * 0.05)(D)),
            "w_gate": _w((lambda *s: torch.randn(*s, dtype=torch.bfloat16) * 0.05)(D, F)),
            "w_up": _w((lambda *s: torch.randn(*s, dtype=torch.bfloat16) * 0.05)(D, F)),
            "w_down": _w((lambda *s: torch.randn(*s, dtype=torch.bfloat16) * 0.05)(F, D)),
        }
    _B_in = _B_setup()

    return {
        "A_q": _A_in["q"],
        "A_k": _A_in["k"],
        "A_v": _A_in["v"],
        "A_shift_k": _A_in["shift_k"],
        "A_ssm_k": _A_in["ssm_k"],
        "A_ssm_D": _A_in["ssm_D"],
        "B_x": _B_in["x"],
        "B_w_r": _B_in["w_r"],
        "B_w_gate": _B_in["w_gate"],
        "B_w_up": _B_in["w_up"],
        "B_w_down": _B_in["w_down"],
    }

def reference(A_q, A_k, A_v, A_shift_k, A_ssm_k, A_ssm_D, B_x, B_w_r, B_w_gate, B_w_up, B_w_down):
    def _A_ref(q, k, v, shift_k, ssm_k, ssm_D):
        B, S, D = q.shape
        ks = shift_k.shape[-1]
        k_pad = _pad(k, ((0, 0), (ks - 1, 0), (0, 0)))
        k_shift = torch.zeros_like(k)
        for t in range(ks):
            k_shift = k_shift + k_pad[:, t:t + S, :] * shift_k[:, ks - 1 - t][None, None, :]
        kv = k_shift * v
        L = 2 * S
        kv_pad = _pad(kv, ((0, 0), (0, S), (0, 0)))
        kf_pad = _pad(ssm_k, ((0, 0), (0, S)))
        kv_f = (lambda x, axes=(-2,-1): torch.fft.rfft2(x.float(), dim=axes))(kv_pad[:, None, :, :], axes=(1, 2))
        kf_f = (lambda x, axes=(-2,-1): torch.fft.rfft2(x.float(), dim=axes))(kf_pad[:, None, :], axes=(1, 2))
        kf_f = (lambda x, *d: x.permute(*d))(kf_f, 1, 2, 0)
        y_f = kv_f * kf_f[None, :, :, :]
        y = (lambda x, s, axes=(-2,-1): torch.fft.irfft2(x, s=s, dim=axes).to(torch.bfloat16))(y_f, (1, L), axes=(1, 2))[:, 0, :S, :]
        y = y + kv * ssm_D[None, None, :]
        return q * y

    def _B_ref(x, w_r, w_gate, w_up, w_down):
        B, S, D = x.shape
        C = 8
        r = x @ w_r
        idx = torch.argsort(-r, axis=-1)[:, :C]
        idx_e = (lambda x, n, axis: torch.repeat_interleave(x, n, dim=axis))(idx.reshape(B, C, 1), D, axis=2)
        x_sel = (lambda x, i, axis: torch.take_along_dim(x, i, dim=axis))(x, idx_e, axis=1)
        g = _silu(x_sel @ w_gate)
        u = x_sel @ w_up
        y = (g * u) @ w_down
        r_sel = _sigmoid((lambda x, i, axis: torch.take_along_dim(x, i, dim=axis))(r, idx, axis=1))
        y = y * r_sel.reshape(B, C, 1)
        b_off = (torch.arange(B) * S).reshape(B, 1)
        flat_idx = (idx + b_off).reshape(B * C)
        zero = (x * 0.0).reshape(B * S, D)
        upd = _scatter_add(zero, flat_idx, y.reshape(B * C, D), axis=0)
        return x + upd.reshape(B, S, D)

    x = A_q
    xn = _layernorm(x, axis=-1)
    return x + _A_ref(xn, A_k, A_v, A_shift_k, A_ssm_k, A_ssm_D) + _B_ref(xn, B_w_r, B_w_gate, B_w_up, B_w_down)
```

```cpp
#include <hip/hip_runtime.h>
#include <hip/hip_bf16.h>
#include <stdint.h>

constexpr int S = 2048, D = 576, F = 4608, C = 8;
typedef unsigned short u16;
__device__ __forceinline__ float bf2f(u16 b) { return __uint_as_float((unsigned)b << 16); }
__device__ __forceinline__ u16 f2bf(float f) { __hip_bfloat16 h = __float2bfloat16(f); return *(u16*)&h; }
__device__ __forceinline__ float rbf(float f) { return bf2f(f2bf(f)); }

__global__ void k1(const u16* q, const u16* k, const u16* v, const u16* sk, const u16* wr, u16* xn, u16* kv, float* r) {
    int t = blockIdx.x; int tid = threadIdx.x;
    __shared__ float red[16];
    float x = bf2f(q[t * D + tid]);
    float s = x;
    for (int m = 32; m; m >>= 1) s += __shfl_xor(s, m);
    if ((tid & 63) == 0) red[tid >> 6] = s;
    __syncthreads();
    float tot = 0; for (int i = 0; i < 9; i++) tot += red[i];
    float mean = tot / D;
    __syncthreads();
    float dd = (x - mean) * (x - mean);
    for (int m = 32; m; m >>= 1) dd += __shfl_xor(dd, m);
    if ((tid & 63) == 0) red[tid >> 6] = dd;
    __syncthreads();
    float vt = 0; for (int i = 0; i < 9; i++) vt += red[i];
    float var = rbf(vt / D);
    float mb = rbf(mean);
    float sd = rbf(sqrtf(rbf(var + 1e-5f)));
    float xnv = rbf(rbf(x - mb) / sd);
    xn[t * D + tid] = f2bf(xnv);
    __syncthreads();
    float p = xnv * bf2f(wr[tid]);
    for (int m = 32; m; m >>= 1) p += __shfl_xor(p, m);
    if ((tid & 63) == 0) red[tid >> 6] = p;
    __syncthreads();
    if (tid == 0) { float rr = 0; for (int i = 0; i < 9; i++) rr += red[i]; r[t] = rbf(rr); }
    float acc = 0;
    for (int j = 0; j < 4; j++) {
        int tt = t + j - 3;
        float kk = tt >= 0 ? bf2f(k[tt * D + tid]) : 0.f;
        acc = rbf(acc + rbf(kk * bf2f(sk[tid * 4 + 3 - j])));
    }
    kv[t * D + tid] = f2bf(acc * bf2f(v[t * D + tid]));
}
__global__ void k2(const u16* q, const u16* xn, const u16* kv, const u16* h, const u16* Dv, u16* out) {
    int t = blockIdx.x; int d = threadIdx.x;
    float acc = 0;
    for (int s = 0; s <= t; s++) acc += bf2f(kv[s * D + d]) * bf2f(h[d * S + t - s]);
    float y = rbf(acc);
    y = rbf(y + rbf(bf2f(kv[t * D + d]) * bf2f(Dv[d])));
    float xnv = bf2f(xn[t * D + d]);
    float A = rbf(xnv * y);
    out[t * D + d] = f2bf(rbf(bf2f(q[t * D + d]) + A));
}
__global__ void k3(const float* r, int* idx) {
    int t = blockIdx.x * 256 + threadIdx.x;
    float my = r[t]; int cnt = 0;
    for (int j = 0; j < S; j++) { float o = r[j]; cnt += (o > my) || (o == my && j < t); }
    if (cnt < C) idx[cnt] = t;
}
__global__ void k4(const u16* xn, const int* idx, const u16* wg, const u16* wu, u16* hact) {
    int f = blockIdx.x * 64 + threadIdx.x; int c = blockIdx.y;
    const u16* xr = xn + idx[c] * D;
    float g = 0, u = 0;
    for (int k = 0; k < D; k++) { float x = bf2f(xr[k]); g += x * bf2f(wg[k * F + f]); u += x * bf2f(wu[k * F + f]); }
    g = rbf(g); u = rbf(u);
    float sg = rbf(g / (1.f + __expf(-g)));
    hact[c * F + f] = f2bf(sg * u);
}
__global__ void k5(const u16* hact, const u16* wd, const float* r, const int* idx, float* moe) {
    int d = blockIdx.x * 64 + threadIdx.x; int c = blockIdx.y;
    float y = 0;
    for (int f = 0; f < F; f++) y += bf2f(hact[c * F + f]) * bf2f(wd[f * D + d]);
    y = rbf(y);
    float rs = rbf(1.f / (1.f + __expf(-r[idx[c]])));
    moe[c * D + d] = rbf(y * rs);
}
__global__ void k6(const u16* xn, const int* idx, const float* moe, u16* out) {
    int t = blockIdx.x; int d = threadIdx.x;
    float upd = 0;
    for (int c = 0; c < C; c++) if (idx[c] == t) upd = moe[c * D + d];
    float B = rbf(bf2f(xn[t * D + d]) + upd);
    out[t * D + d] = f2bf(bf2f(out[t * D + d]) + B);
}

extern "C" void kernel_launch(void* const* d_in, const int* in_sizes, int n_in,
                              void* d_out, int out_size, void* d_ws, size_t ws_size,
                              hipStream_t stream) {
    const u16* q = (const u16*)d_in[0]; const u16* k = (const u16*)d_in[1]; const u16* v = (const u16*)d_in[2];
    const u16* sk = (const u16*)d_in[3]; const u16* h = (const u16*)d_in[4]; const u16* Dv = (const u16*)d_in[5];
    const u16* wr = (const u16*)d_in[7]; const u16* wg = (const u16*)d_in[8]; const u16* wu = (const u16*)d_in[9]; const u16* wd = (const u16*)d_in[10];
    char* ws = (char*)d_ws;
    u16* xn = (u16*)ws; u16* kv = (u16*)(ws + (4 << 20)); float* r = (float*)(ws + (8 << 20)); int* idx = (int*)(ws + (9 << 20));
    u16* hact = (u16*)(ws + (10 << 20)); float* moe = (float*)(ws + (11 << 20));
    u16* out = (u16*)d_out;
    k1<<<S, D, 0, stream>>>(q, k, v, sk, wr, xn, kv, r);
    k2<<<S, D, 0, stream>>>(q, xn, kv, h, Dv, out);
    k3<<<S / 256, 256, 0, stream>>>(r, idx);
    k4<<<dim3(F / 64, C), 64, 0, stream>>>(xn, idx, wg, wu, hact);
    k5<<<dim3(D / 64, C), 64, 0, stream>>>(hact, wd, r, idx, moe);
    k6<<<S, D, 0, stream>>>(xn, idx, moe, out);
}
```

```cpp
#include <hip/hip_runtime.h>
#include <stdint.h>

typedef unsigned short u16;
typedef unsigned u32;
typedef unsigned long long u64;
typedef u32 v4u __attribute__((ext_vector_type(4)));
typedef u32 v2u __attribute__((ext_vector_type(2)));
typedef float f4 __attribute__((ext_vector_type(4)));
typedef float f2 __attribute__((ext_vector_type(2)));
typedef __bf16 bf8 __attribute__((ext_vector_type(8)));
typedef __bf16 bf2 __attribute__((ext_vector_type(2)));
#define GAS __attribute__((address_space(1)))
#define LAS __attribute__((address_space(3)))
#define RLX_AGENT __ATOMIC_RELAXED, __HIP_MEMORY_SCOPE_AGENT
typedef __amdgpu_buffer_rsrc_t rsrc_t;

#define AUX_IN 0
#define AUX_W 2
#define AUX_OUT 0
#define WS_AT_END 1
#define U_DELAY 0
#define Y_DELAY 0

constexpr int S = 2048, D = 576, F = 4608;
constexpr int NT = 640;
constexpr u32 SENT = 0xFFFFFFFFu;
constexpr int SC1 = 16;
constexpr u32 SPIN_CAP = 1u << 16;

constexpr int W_E = 0, W_F1 = 4096, W_F2 = W_F1 + 256 * 128, W_KEY = W_F2 + 256 * 128, W_SEL = W_KEY + 256 * 64, W_XSEL = W_SEL + 128,
              W_U = W_XSEL + 9216 + 896, W_Y = W_U + 256 * 9216, W_PART = W_Y + 144 * 16384, W_END = W_PART + 72 * 18432, W_DBG = W_END;
constexpr int WS_BYTES = W_END + (1 << 20);
static_assert(W_U % 128 == 0 && W_XSEL % 128 == 0, "align");
constexpr int HC = 4160, HCH = 4 * HC, L_H = 0, UCH = 290 * 16, L_U = L_H + 4 * HCH, L_Y = L_U + 4 * UCH, L_ST = L_Y + 16384, L_C = L_ST + 320 * 80,
              L_STAT = L_C + 64, L_SEL = L_STAT + 64, L_WM = L_SEL + 64, L_RED = L_WM + 64, L_END = L_RED + 4 * 128;
constexpr int L_R = 0, L_HA = 9 * 8 * 128 * 4, HAS = 144, L_XS = L_HA + 8 * HAS, XSS = 1168;
constexpr int L_LIST = 0;
enum { C_CLR = 0, C_F1 = 1, C_F2 = 2, C_SEL = 3, C_UST = 4, C_CNT = 5 };

struct Params {
    const u16 *q, *k, *v, *sk, *h, *dv, *wr, *wg, *wu, *wd;
    u16* out;
    char* ws;
};

__device__ __forceinline__ float asf(u32 x) { return __builtin_bit_cast(float, x); }
__device__ __forceinline__ u32 asu(float x) { return __builtin_bit_cast(u32, x); }
__device__ __forceinline__ float lo16(u32 x) { return asf(x << 16); }
__device__ __forceinline__ float hi16(u32 x) { return asf(x & 0xFFFF0000u); }
__device__ __forceinline__ f2 unpk(u32 x) { return f2{lo16(x), hi16(x)}; }
__device__ __forceinline__ u32 pk(float a, float b) { return __builtin_bit_cast(u32, __builtin_convertvector(f2{a, b}, bf2)); }
__device__ __forceinline__ float rbf(float a) { return lo16(pk(a, a)); }
__device__ __forceinline__ f2 rbf2(f2 a) { return unpk(pk(a.x, a.y)); }
__device__ __forceinline__ u32 nos(u32 x) { return x < 0xFFFFFFFEu ? x : 0xFFFFFFFEu; }
__device__ __forceinline__ bool valid4(v4u v) { return v.x != SENT && v.y != SENT && v.z != SENT && v.w != SENT; }
__device__ __forceinline__ rsrc_t mkrs(const void* p, u32 bytes) { return __builtin_amdgcn_make_buffer_rsrc((void*)p, 0, bytes, 0x00020000); }
template <int AUX> __device__ __forceinline__ u32 ld32(rsrc_t r, u32 vo, u32 so) { return __builtin_amdgcn_raw_buffer_load_b32(r, vo, so, AUX); }
template <int AUX> __device__ __forceinline__ v2u ld64(rsrc_t r, u32 vo, u32 so) { return __builtin_amdgcn_raw_buffer_load_b64(r, vo, so, AUX); }
template <int AUX> __device__ __forceinline__ v4u ld128(rsrc_t r, u32 vo, u32 so) { return __builtin_amdgcn_raw_buffer_load_b128(r, vo, so, AUX); }
template <int AUX> __device__ __forceinline__ void st32(u32 v, rsrc_t r, u32 vo, u32 so) { __builtin_amdgcn_raw_buffer_store_b32(v, r, vo, so, AUX); }
template <int AUX> __device__ __forceinline__ void st64(v2u v, rsrc_t r, u32 vo, u32 so) { __builtin_amdgcn_raw_buffer_store_b64(v, r, vo, so, AUX); }
template <int AUX> __device__ __forceinline__ void st128(v4u v, rsrc_t r, u32 vo, u32 so) { __builtin_amdgcn_raw_buffer_store_b128(v, r, vo + so, 0, AUX); }
#define FENCE() asm volatile("" ::: "memory")
#define DRAIN() asm volatile("s_waitcnt vmcnt(0)" ::: "memory")
__device__ __forceinline__ void bar() {
    __builtin_amdgcn_sched_barrier(0);
    asm volatile("s_waitcnt lgkmcnt(0)" ::: "memory");
    __builtin_amdgcn_s_barrier();
    FENCE();
    __builtin_amdgcn_sched_barrier(0);
}
template <int CTRL> __device__ __forceinline__ u32 dpp(u32 x) { return (u32)__builtin_amdgcn_update_dpp(0, (int)x, CTRL, 0xF, 0xF, true); }
__device__ __forceinline__ float sum32(float x) {
    x += asf(dpp<0xB1>(asu(x)));
    x += asf(dpp<0x4E>(asu(x)));
    x += asf(dpp<0x141>(asu(x)));
    x += asf(dpp<0x140>(asu(x)));
    auto r = __builtin_amdgcn_permlane16_swap(asu(x), asu(x), false, false);
    return asf(r[0]) + asf(r[1]);
}
__device__ __forceinline__ u32 umax(u32 a, u32 b) { return a > b ? a : b; }
__device__ __forceinline__ u32 umin(u32 a, u32 b) { return a < b ? a : b; }
__device__ __forceinline__ u32 max64(u32 x) {
    x = umax(x, dpp<0xB1>(x));
    x = umax(x, dpp<0x4E>(x));
    x = umax(x, dpp<0x141>(x));
    x = umax(x, dpp<0x140>(x));
    auto r = __builtin_amdgcn_permlane16_swap(x, x, false, false);
    x = umax(r[0], r[1]);
    auto s = __builtin_amdgcn_permlane32_swap(x, x, false, false);
    return umax(s[0], s[1]);
}
__device__ __forceinline__ f4 mfma(v4u a, v4u b, f4 c) {
    return __builtin_amdgcn_mfma_f32_16x16x32_bf16(__builtin_bit_cast(bf8, a), __builtin_bit_cast(bf8, b), c, 0, 0, 0);
}

#define STAMP(i) do { } while (0)

__shared__ __attribute__((aligned(16))) char lds_raw[L_END];

__device__ __forceinline__ LAS char* L(int off) { return (LAS char*)lds_raw + off; }
__device__ __forceinline__ void cset(int w) { *(volatile LAS u32*)L(L_C + 4 * w) = 1u; }
__device__ __forceinline__ void cwait(int w) {
    for (u32 n = 0; n < (1u << 22); ++n) {
        if (*(volatile LAS u32*)L(L_C + 4 * w)) break;
        __builtin_amdgcn_s_sleep(1);
    }
    FENCE();
}
__device__ __forceinline__ void wait_flags(rsrc_t rw, int base, u32 tlo, u32 thi, int lane) {
    for (u32 n = 0; n < SPIN_CAP; ++n) {
        FENCE();
        bool ok = true;
#pragma unroll
        for (int i = 0; i < 4; ++i) {
            const v2u f = ld64<SC1>(rw, (lane + 64 * i) * 128, base);
            ok &= f.x == tlo && f.y == thi;
        }
        if (__all(ok)) break;
        __builtin_amdgcn_s_sleep(4);
    }
    FENCE();
}
__device__ __forceinline__ void wait_word(rsrc_t rw, u32 off) {
    for (u32 n = 0; n < SPIN_CAP; ++n) {
        FENCE();
        if (ld32<SC1>(rw, 0, off) != SENT) break;
        __builtin_amdgcn_s_sleep(2);
    }
    FENCE();
}
template <int N> __device__ __forceinline__ void poll16(rsrc_t rw, const u32 (&vo)[N], u32 so, v4u (&v)[N]) {
    bool ok[N];
#pragma unroll
    for (int i = 0; i < N; ++i) ok[i] = false;
    for (u32 n = 0; n < SPIN_CAP; ++n) {
        FENCE();
#pragma unroll
        for (int i = 0; i < N; ++i) if (!ok[i]) v[i] = ld128<SC1>(rw, vo[i], so);
        bool all = true;
#pragma unroll
        for (int i = 0; i < N; ++i) { ok[i] = valid4(v[i]); all &= ok[i]; }
        if (__all(all)) break;
    }
    FENCE();
}

#define DSR64(dst, addr, off) asm volatile("ds_read_b64 %0, %1 offset:%2" : "=v"(dst) : "v"(addr), "i"(off))
#define DSR128(dst, addr, off) asm volatile("ds_read_b128 %0, %1 offset:%2" : "=v"(dst) : "v"(addr), "i"(off))
#define SB() __builtin_amdgcn_sched_barrier(0)
#define MFMA_IP(acc, a, b) asm volatile("v_mfma_f32_16x16x32_bf16 %0, %1, %2, %0" : "+v"(acc) : "v"(a), "v"(b))

struct AF { v2u lo, hi; };
__device__ __forceinline__ v4u cat(const AF& a) { return v4u{a.lo.x, a.lo.y, a.hi.x, a.hi.y}; }

template <int I0> __device__ __forceinline__ void conv_phase(u32 va, u32 vb, f4 (&acc)[8], AF (&alo)[4], v4u (&blo)[4], AF (&alo2)[4], v4u (&blo2)[4]) {
    AF ahi[4];
    v4u bhi[4];
    constexpr int OA = 64 * I0, OB = 448 - 64 * I0;
    asm volatile("s_waitcnt lgkmcnt(0)" : "+v"(alo[0].lo), "+v"(alo[0].hi), "+v"(alo[1].lo), "+v"(alo[1].hi), "+v"(alo[2].lo), "+v"(alo[2].hi), "+v"(alo[3].lo),
                 "+v"(alo[3].hi), "+v"(blo[0]), "+v"(blo[1]), "+v"(blo[2]), "+v"(blo[3]));
    SB();
#pragma unroll
    for (int n = 0; n < 16; ++n) {
        const int e = n >> 2, b = n & 3;
        MFMA_IP(acc[e + b], cat(alo[e]), blo[b]);
        if (n < 4) DSR128(bhi[n], vb, OB + 512 * (4 + n));
        else if (n < 12) {
            if (n & 1) DSR64(ahi[(n - 4) >> 1].hi, va, OA + 512 * (4 + ((n - 4) >> 1)) + 8);
            else DSR64(ahi[(n - 4) >> 1].lo, va, OA + 512 * (4 + ((n - 4) >> 1)));
        }
        SB();
    }
    asm volatile("s_waitcnt lgkmcnt(0)" : "+v"(ahi[0].lo), "+v"(ahi[0].hi), "+v"(ahi[1].lo), "+v"(ahi[1].hi), "+v"(ahi[2].lo), "+v"(ahi[2].hi), "+v"(ahi[3].lo),
                 "+v"(ahi[3].hi), "+v"(bhi[0]), "+v"(bhi[1]), "+v"(bhi[2]), "+v"(bhi[3]));
    SB();
    int n = 0;
#pragma unroll
    for (int half = 0; half < 2; ++half)
#pragma unroll
        for (int e = 0; e < 4; ++e)
#pragma unroll
            for (int b = 0; b < 4 - e; ++b) {
                if (half == 0) MFMA_IP(acc[4 + e + b], cat(alo[e]), bhi[b]);
                else MFMA_IP(acc[4 + e + b], cat(ahi[e]), blo[b]);
                if (I0 < 7) {
                    if (n < 4) DSR128(blo2[n], vb, OB - 64 + 512 * n);
                    else if (n < 12) {
                        if (n & 1) DSR64(alo2[(n - 4) >> 1].hi, va, OA + 64 + 512 * ((n - 4) >> 1) + 8);
                        else DSR64(alo2[(n - 4) >> 1].lo, va, OA + 64 + 512 * ((n - 4) >> 1));
                    }
                }
                ++n;
                SB();
            }
}

__global__ void __launch_bounds__(NT, 3) fused_kernel(Params p) {
    const int tid = threadIdx.x, lane = tid & 63, wave = __builtin_amdgcn_readfirstlane(tid >> 6);
    const int wg = blockIdx.x, xq = wg & 7, xi = wg >> 3, b = 32 * xq + xi;
    const bool isConv = xi < 18, isMoe = xi >= 18 && xi < 27;
    const int ridx = 5 * xq + xi - 27;
    const bool isRed = xi >= 27 && ridx < 36, isSel = xi >= 27 && ridx == 39;
    const int cj = 18 * xq + xi, mu = 9 * xq + xi - 18;
    constexpr int SELWG = 255;
    STAMP(0);
    asm volatile("" ::"s"(p.q), "s"(p.k), "s"(p.v), "s"(p.sk), "s"(p.h), "s"(p.dv), "s"(p.wr), "s"(p.wg), "s"(p.wu), "s"(p.wd), "s"(p.out), "s"(p.ws));
    const rsrc_t rw = mkrs(p.ws, WS_BYTES);
    const rsrc_t rq = mkrs(p.q, S * D * 2);

    if (wave == 9) {
        const v4u s4 = {SENT, SENT, SENT, SENT};
        const v2u e = ld64<SC1>(rw, 0, W_E);
#pragma unroll
        for (int i = 0; i < 9; ++i) st128<SC1>(s4, rw, lane * 16 + 1024 * i, W_U + b * 9216);
        if (lane < 4) st128<SC1>(s4, rw, lane * 16, W_KEY + b * 64);
        if (isSel) {
#pragma unroll
            for (int i = 0; i < 9; ++i) st128<SC1>(s4, rw, lane * 16 + 1024 * i, W_XSEL);
            if (lane < 4) st128<SC1>(s4, rw, lane * 16, W_SEL);
        }
        if (lane < 16) *(LAS u32*)L(L_C + 4 * lane) = 0u;
        bar();
        const u64 tag = (((u64)e.y << 32) | e.x) + 1ull;
        const u32 tlo = __builtin_amdgcn_readfirstlane((u32)tag), thi = __builtin_amdgcn_readfirstlane((u32)(tag >> 32));
        DRAIN();
        STAMP(1);
        if (lane == 0) st64<SC1>(v2u{tlo, thi}, rw, 0, W_F1 + wg * 128);
        cset(C_CLR);
        if (isConv) {
            wait_flags(rw, W_F1, tlo, thi, lane);
            cset(C_F1);
            STAMP(2);
#pragma unroll
            for (int i = 0; i < 16; ++i) st128<SC1>(s4, rw, lane * 16 + 1024 * i, W_Y + cj * 16384);
            bar();
            DRAIN();
            if (lane == 0) st64<SC1>(v2u{tlo, thi}, rw, 0, W_F2 + wg * 128);
            STAMP(3);
            wait_flags(rw, W_F2, tlo, thi, lane);
            cset(C_F2);
            STAMP(4);
            bar();
        } else if (isMoe) {
#pragma unroll
            for (int i = 0; i < 18; ++i) st128<SC1>(s4, rw, lane * 16 + 1024 * i, W_PART + mu * 18432);
            DRAIN();
            if (lane == 0) st64<SC1>(v2u{tlo, thi}, rw, 0, W_F2 + wg * 128);
            STAMP(2);
            for (u32 n = 0; n < SPIN_CAP; ++n) {
                FENCE();
                const v2u f = ld64<SC1>(rw, 0, W_F1 + SELWG * 128);
                if (f.x == tlo && f.y == thi) break;
                __builtin_amdgcn_s_sleep(4);
            }
            wait_word(rw, W_XSEL + 9212);
            STAMP(3);
            u32 vo[9];
            v4u xv[9];
#pragma unroll
            for (int i = 0; i < 9; ++i) vo[i] = (lane + 64 * i) * 16;
            poll16<9>(rw, vo, W_XSEL, xv);
#pragma unroll
            for (int i = 0; i < 9; ++i) {
                const int c = lane + 64 * i;
                *(LAS v4u*)L(L_XS + (c / 72) * XSS + (c % 72) * 16) = xv[i];
            }
            STAMP(4);
            bar();
            bar();
            bar();
            wait_flags(rw, W_F2, tlo, thi, lane);
            cset(C_F2);
        } else {
            if (lane == 0) st64<SC1>(v2u{tlo, thi}, rw, 0, W_F2 + wg * 128);
            if (isSel) {
                wait_flags(rw, W_F1, tlo, thi, lane);
                if (lane == 0) st64<SC1>(v2u{tlo, thi}, rw, 0, W_E);
                STAMP(2);
                bar();
                bar();
                bar();
                bar();
            } else {
                bar();
            }
            wait_flags(rw, W_F2, tlo, thi, lane);
            cset(C_F2);
            STAMP(4);
        }
        if (!isSel) {
            u32 vo[1] = {(u32)(lane & 3) * 16};
            v4u sv[1];
            poll16<1>(rw, vo, W_SEL, sv);
            if (lane < 4) *(LAS v4u*)L(L_SEL + lane * 16) = sv[0];
            asm volatile("s_waitcnt lgkmcnt(0)" ::: "memory");
        }
        cset(C_SEL);
        STAMP(5);
    } else {
        const bool isK = wave < 5;
        const int cp = tid;
        const int lw = wave - 5;
        v4u hd[4];
        v2u hn[4];
        if (isK) {
            const rsrc_t rk = mkrs(p.k, S * D * 2), rv = mkrs(p.v, S * D * 2);
            const u32 vo = tid < 288 ? tid * 4 : 0x40000000u;
            u32 kk[11], vv[8], qq[8];
            v4u kvc[2];
#pragma unroll
            for (int i = 0; i < 11; ++i) {
                const int row = 8 * b - 3 + i;
                kk[i] = ld32<AUX_IN>(rk, vo, row >= 0 ? row * 1152 : 0x40000000);
            }
#pragma unroll
            for (int i = 0; i < 8; ++i) vv[i] = ld32<AUX_IN>(rv, vo, (8 * b + i) * 1152);
            const v4u sk = ld128<0>(mkrs(p.sk, D * 8), tid * 16, 0);
#pragma unroll
            for (int i = 0; i < 8; ++i) qq[i] = ld32<AUX_IN>(rq, vo, (8 * b + i) * 1152);
            const u32 dvp = ld32<0>(mkrs(p.dv, D * 2), vo, 0);
            bar();
            const f2 w0 = {lo16(sk.x), lo16(sk.z)}, w1 = {hi16(sk.x), hi16(sk.z)}, w2 = {lo16(sk.y), lo16(sk.w)}, w3 = {hi16(sk.y), hi16(sk.w)};
            f2 kf[11], r[8];
#pragma unroll
            for (int i = 0; i < 11; ++i) kf[i] = unpk(kk[i]);
#pragma unroll
            for (int t = 0; t < 8; ++t) r[t] = (kf[t] * w3 + kf[t + 1] * w2 + kf[t + 2] * w1 + kf[t + 3] * w0) * unpk(vv[t]);
            kvc[0] = v4u{nos(pk(r[7].x, r[6].x)), nos(pk(r[5].x, r[4].x)), nos(pk(r[3].x, r[2].x)), nos(pk(r[1].x, r[0].x))};
            kvc[1] = v4u{nos(pk(r[7].y, r[6].y)), nos(pk(r[5].y, r[4].y)), nos(pk(r[3].y, r[2].y)), nos(pk(r[1].y, r[0].y))};
            STAMP(1);
            cwait(C_CLR);
            if (tid < 288) {
                st128<SC1>(kvc[0], rw, tid * 16, W_U + b * 9216);
                st128<SC1>(kvc[1], rw, tid * 16, W_U + b * 9216 + 4608);
            }
            cset(C_UST);
            STAMP(2);
            *(LAS v4u*)L(L_ST + tid * 80) = v4u{qq[0], qq[1], qq[2], qq[3]};
            *(LAS v4u*)L(L_ST + tid * 80 + 16) = v4u{qq[4], qq[5], qq[6], qq[7]};
            *(LAS v4u*)L(L_ST + tid * 80 + 32) = kvc[0];
            *(LAS v4u*)L(L_ST + tid * 80 + 48) = kvc[1];
            *(LAS u32*)L(L_ST + tid * 80 + 64) = dvp;
        } else {
            const int hf = lane >> 5, l32 = lane & 31, tl = 2 * lw + hf, tok = 8 * b + tl;
            const rsrc_t rr = mkrs(p.wr, D * 2);
            u32 xq9[9], wr9[9];
#pragma unroll
            for (int i = 0; i < 9; ++i) xq9[i] = ld32<AUX_IN>(rq, tok * 1152 + l32 * 4, 128 * i);
#pragma unroll
            for (int i = 0; i < 9; ++i) wr9[i] = ld32<0>(rr, l32 * 4, 128 * i);
            {
                const int c = 4 * cj + lw, d = c < 288 ? 2 * c : 2 * (c - 288) + 1;
                const rsrc_t rh = mkrs(p.h, isConv ? D * S * 2 : 0);
#pragma unroll
                for (int i = 0; i < 4; ++i) hd[i] = ld128<AUX_IN>(rh, lane * 16 + 1024 * i, d * 4096);
#pragma unroll
                for (int i = 0; i < 4; ++i) hn[i] = ld64<AUX_IN>(rh, lane * 16 + 1024 * i + 16, d * 4096);
            }
            bar();
            f2 x[9];
            f2 s2 = {0.f, 0.f};
#pragma unroll
            for (int i = 0; i < 9; ++i) { x[i] = unpk(xq9[i]); s2 += x[i]; }
            const float mean = sum32(s2.x + s2.y) * (1.0f / D);
            f2 v2 = {0.f, 0.f};
#pragma unroll
            for (int i = 0; i < 9; ++i) { const f2 dl = x[i] - mean; v2 += dl * dl; }
            const float var = rbf(sum32(v2.x + v2.y) * (1.0f / D));
            const float mb = rbf(mean), sd = rbf(__builtin_amdgcn_sqrtf(rbf(var + 1e-5f)));
            const float inv = __builtin_amdgcn_rcpf(sd);
            f2 a2 = {0.f, 0.f};
#pragma unroll
            for (int i = 0; i < 9; ++i) a2 += rbf2(rbf2(x[i] - mb) * inv) * unpk(wr9[i]);
            const float rs = sum32(a2.x + a2.y);
            const u32 rb = pk(rs, rs) & 0xFFFFu;
            u32 ord = (rb & 0x8000u) ? (~rb & 0xFFFFu) : (rb | 0x8000u);
            if ((rb & 0x7FFFu) > 0x7F80u) ord = 0u;
            const v2u rec = {(ord << 16) | (u32)(2047 - tok), nos(pk(mb, sd))};
            STAMP(1);
            cwait(C_CLR);
            if (l32 == 0) {
                st64<SC1>(rec, rw, tl * 8, W_KEY + b * 64);
                *(LAS f2*)L(L_STAT + tl * 8) = f2{mb, inv};
            }
            STAMP(2);
        }

        if (isConv) {
            if (isK) {
                bar();
            } else {
                LAS char* hb = L(L_H + lw * HCH);
#pragma unroll
                for (int i = 0; i < 4; ++i) {
                    const v4u dd = hd[i];
                    const v2u nn = hn[i];
                    const u32 a01 = __builtin_amdgcn_alignbit(dd.y, dd.x, 16), a12 = __builtin_amdgcn_alignbit(dd.z, dd.y, 16),
                              a23 = __builtin_amdgcn_alignbit(dd.w, dd.z, 16), a3n = __builtin_amdgcn_alignbit(nn.x, dd.w, 16),
                              ann = __builtin_amdgcn_alignbit(nn.y, nn.x, 16);
                    LAS char* o = hb + (lane + 64 * i + 2) * 16;
                    *(LAS v4u*)(o) = dd;
                    *(LAS v4u*)(o + HC) = v4u{a01, a12, a23, a3n};
                    *(LAS v4u*)(o + 2 * HC) = v4u{dd.y, dd.z, dd.w, nn.x};
                    *(LAS v4u*)(o + 3 * HC) = v4u{a12, a23, a3n, ann};
                }
                {
                    const u32 h0 = __builtin_amdgcn_readfirstlane(hd[0].x), h1 = __builtin_amdgcn_readfirstlane(hd[0].y);
                    if (lane < 8) {
                        const int sg = lane >> 1;
                        v4u z = {0u, 0u, 0u, 0u};
                        if (lane & 1) {
                            if (sg == 1) z.w = h0 << 16;
                            else if (sg == 2) z.w = h0;
                            else if (sg == 3) { z.z = h0 << 16; z.w = (h0 >> 16) | (h1 << 16); }
                        }
                        *(LAS v4u*)(hb + sg * HC + (lane & 1) * 16) = z;
                    }
                    if (lane < 32) *(LAS v4u*)L(L_U + lw * UCH + lane * 16) = v4u{0u, 0u, 0u, 0u};
                }
                f4 acc[8];
#pragma unroll
                for (int i = 0; i < 8; ++i) {
                    acc[i] = f4{0.f, 0.f, 0.f, 0.f};
                    asm volatile("" : "+v"(acc[i]));
                }
                STAMP(3);
                cwait(C_F1);
                cwait(C_UST);
                if (U_DELAY) __builtin_amdgcn_s_sleep(U_DELAY);
                STAMP(4);
                {
                    const int l = tid - 320;
                    u32 vo[4];
                    v4u uv[4];
#pragma unroll
                    for (int i = 0; i < 4; ++i) vo[i] = (64 * i + (l >> 2)) * 9216 + (l & 3) * 16;
                    poll16<4>(rw, vo, W_U + cj * 64, uv);
#pragma unroll
                    for (int i = 0; i < 4; ++i) *(LAS v4u*)L(L_U + (l & 3) * UCH + (32 + 64 * i + (l >> 2)) * 16) = uv[i];
                }
                STAMP(5);
                bar();
                const int m1 = (lane & 15) + 1, kq = lane >> 4;
                const u32 va = (u32)(uintptr_t)(hb + (m1 & 3) * HC + 16 * kq + 8 * (m1 >> 2));
                const u32 vb = (u32)(uintptr_t)L(L_U + lw * UCH + (33 + 2 * (lane & 15) - kq) * 16 - 448);
                AF a0[4], a1[4];
                v4u b0[4], b1[4];
#pragma unroll
                for (int n = 0; n < 4; ++n) {
                    DSR128(b0[n], vb, 448 + 512 * n);
                    DSR64(a0[n].lo, va, 512 * n);
                    DSR64(a0[n].hi, va, 512 * n + 8);
                }
                conv_phase<0>(va, vb, acc, a0, b0, a1, b1);
                conv_phase<1>(va, vb, acc, a1, b1, a0, b0);
                conv_phase<2>(va, vb, acc, a0, b0, a1, b1);
                conv_phase<3>(va, vb, acc, a1, b1, a0, b0);
                conv_phase<4>(va, vb, acc, a0, b0, a1, b1);
                conv_phase<5>(va, vb, acc, a1, b1, a0, b0);
                conv_phase<6>(va, vb, acc, a0, b0, a1, b1);
                conv_phase<7>(va, vb, acc, a1, b1, a0, b0);
                asm volatile("s_nop 7\n\ts_nop 1" : "+v"(acc[0]), "+v"(acc[1]), "+v"(acc[2]), "+v"(acc[3]), "+v"(acc[4]), "+v"(acc[5]), "+v"(acc[6]), "+v"(acc[7]));
                STAMP(6);
#pragma unroll
                for (int q = 0; q < 8; q += 2) {
                    const u32 y0 = nos(pk(acc[q][0], acc[q][1])), y1 = nos(pk(acc[q][2], acc[q][3]));
                    const u32 z0 = nos(pk(acc[q + 1][0], acc[q + 1][1])), z1 = nos(pk(acc[q + 1][2], acc[q + 1][3]));
                    const auto s0 = __builtin_amdgcn_permlane16_swap(y0, z0, false, false);
                    const auto s1 = __builtin_amdgcn_permlane16_swap(y1, z1, false, false);
                    const int blk = 32 * (q + (kq & 1)) + 2 * (lane & 15) + (kq >> 1);
                    *(LAS v4u*)L(L_Y + blk * 64 + ((lw ^ (blk >> 1)) & 3) * 16) = v4u{s0[0], s1[0], s0[1], s1[1]};
                }
                bar();
                {
                    const int l = tid - 320;
#pragma unroll
                    for (int i = 0; i < 4; ++i) {
                        const int c = l + 256 * i, blk = c >> 2;
                        const v4u yv = *(LAS v4u*)L(L_Y + blk * 64 + (((c & 3) ^ (blk >> 1)) & 3) * 16);
                        st128<SC1>(yv, rw, c * 16, W_Y + cj * 16384);
                    }
                }
                STAMP(7);
            }
        } else if (isMoe) {
            const int c = lane & 15, kq = lane >> 4;
            const GAS char* gp = (const GAS char*)(c < 8 ? p.wg : p.wu) + (size_t)(64 * wave + 8 * kq) * (F * 2) + mu * 128 + (c & 7) * 16;
            v4u gr[2][8];
            v2u dr[2][8];
#pragma unroll
            for (int ks = 0; ks < 2; ++ks)
#pragma unroll
                for (int e = 0; e < 8; ++e) {
                    const GAS v4u* a = (const GAS v4u*)(gp + (size_t)(32 * ks + e) * (F * 2));
                    gr[ks][e] = AUX_W ? __builtin_nontemporal_load(a) : *a;
                }
            const rsrc_t rd = mkrs(p.wd, F * D * 2);
#pragma unroll
            for (int ks = 0; ks < 2; ++ks)
#pragma unroll
                for (int e = 0; e < 8; ++e) dr[ks][e] = ld64<AUX_W>(rd, kq * (8 * 1152) + c * 8, (64 * mu + 32 * ks + e) * 1152 + wave * 128);
            STAMP(3);
            v4u gf[2][8];
#pragma unroll
            for (int ks = 0; ks < 2; ++ks)
#pragma unroll
                for (int nn = 0; nn < 8; ++nn)
#pragma unroll
                    for (int pp = 0; pp < 4; ++pp)
                        gf[ks][nn][pp] = __builtin_amdgcn_perm(gr[ks][2 * pp + 1][nn >> 1], gr[ks][2 * pp][nn >> 1], (nn & 1) ? 0x07060302u : 0x05040100u);
#pragma unroll
            for (int ks = 0; ks < 2; ++ks)
#pragma unroll
                for (int nn = 0; nn < 8; ++nn) asm volatile("" : "+v"(gf[ks][nn]));
            STAMP(4);
            bar();
            STAMP(5);
            {
                v4u xa[2];
#pragma unroll
                for (int ks = 0; ks < 2; ++ks) {
                    xa[ks] = v4u{0u, 0u, 0u, 0u};
                    if (c < 8) xa[ks] = *(LAS v4u*)L(L_XS + c * XSS + (64 * wave + 32 * ks + 8 * kq) * 2);
                }
                f4 g[8];
#pragma unroll
                for (int nn = 0; nn < 8; ++nn) g[nn] = mfma(xa[1], gf[1][nn], mfma(xa[0], gf[0][nn], f4{0.f, 0.f, 0.f, 0.f}));
                if (kq < 2) {
#pragma unroll
                    for (int r = 0; r < 4; ++r) {
                        LAS char* o = L(L_R + ((wave * 8 + 4 * kq + r) * 128 + 8 * c) * 4);
                        *(LAS f4*)o = f4{g[0][r], g[1][r], g[2][r], g[3][r]};
                        *(LAS f4*)(o + 16) = f4{g[4][r], g[5][r], g[6][r], g[7][r]};
                    }
                }
            }
            bar();
            if (wave < 8) {
                float g = 0.f, u = 0.f;
#pragma unroll
                for (int w = 0; w < 9; ++w) {
                    g += *(LAS float*)L(L_R + ((w * 8 + wave) * 128 + lane) * 4);
                    u += *(LAS float*)L(L_R + ((w * 8 + wave) * 128 + 64 + lane) * 4);
                }
                g = rbf(g);
                u = rbf(u);
                const float sg = rbf(g * __builtin_amdgcn_rcpf(1.0f + __expf(-g)));
                *(LAS u16*)L(L_HA + wave * HAS + lane * 2) = (u16)pk(sg * u, 0.f);
            }
            v4u df[2][4];
#pragma unroll
            for (int ks = 0; ks < 2; ++ks)
#pragma unroll
                for (int nn = 0; nn < 4; ++nn)
#pragma unroll
                    for (int pp = 0; pp < 4; ++pp)
                        df[ks][nn][pp] = __builtin_amdgcn_perm(dr[ks][2 * pp + 1][nn >> 1], dr[ks][2 * pp][nn >> 1], (nn & 1) ? 0x07060302u : 0x05040100u);
            bar();
            STAMP(6);
            {
                v4u ha[2];
#pragma unroll
                for (int ks = 0; ks < 2; ++ks) {
                    ha[ks] = v4u{0u, 0u, 0u, 0u};
                    if (c < 8) ha[ks] = *(LAS v4u*)L(L_HA + c * HAS + (32 * ks + 8 * kq) * 2);
                }
                f4 y[4];
#pragma unroll
                for (int nn = 0; nn < 4; ++nn) y[nn] = mfma(ha[1], df[1][nn], mfma(ha[0], df[0][nn], f4{0.f, 0.f, 0.f, 0.f}));
                if (kq < 2) {
#pragma unroll
                    for (int r = 0; r < 4; ++r)
                        st128<SC1>(v4u{nos(asu(y[0][r])), nos(asu(y[1][r])), nos(asu(y[2][r])), nos(asu(y[3][r]))}, rw, (4 * kq + r) * 2304 + c * 16,
                                   W_PART + mu * 18432 + wave * 256);
                }
            }
            STAMP(7);
        } else if (isSel) {
            if (tid == 0) *(LAS u32*)L(L_C + 4 * C_CNT) = 0u;
            bar();
            const int sw = wave < 4 ? wave : wave - 1;
            v4u kv2[2];
            if (wave != 4) {
                u32 vo[2] = {(u32)(sw * 64 + lane) * 16, (u32)(sw * 64 + lane) * 16 + 8192};
                poll16<2>(rw, vo, W_KEY, kv2);
                const u32 mx = max64(umax(umax(kv2[0].x, kv2[0].z), umax(kv2[1].x, kv2[1].z)));
                if (lane == 0) *(LAS u32*)L(L_WM + 4 * sw) = mx;
            }
            STAMP(3);
            bar();
            if (wave != 4) {
                const v4u m0 = *(LAS v4u*)L(L_WM), m1 = *(LAS v4u*)L(L_WM + 16);
                const u32 thr = umin(umin(umin(m0.x, m0.y), umin(m0.z, m0.w)), umin(umin(m1.x, m1.y), umin(m1.z, m1.w)));
#pragma unroll
                for (int i = 0; i < 4; ++i) {
                    const u32 key = (i & 1) ? kv2[i >> 1].z : kv2[i >> 1].x, ms = (i & 1) ? kv2[i >> 1].w : kv2[i >> 1].y;
                    if (key >= thr) {
                        const u32 pos = __hip_atomic_fetch_add((LAS u32*)L(L_C + 4 * C_CNT), 1u, __ATOMIC_RELAXED, __HIP_MEMORY_SCOPE_WORKGROUP);
                        *(LAS v2u*)L(L_LIST + pos * 8) = v2u{key, ms};
                    }
                }
            }
            bar();
            {
                const int n = *(LAS u32*)L(L_C + 4 * C_CNT);
                for (int i = tid; i < n; i += 576) {
                    const v2u me = *(LAS v2u*)L(L_LIST + i * 8);
                    int rank = 0;
                    for (int j = 0; j < n; ++j) rank += *(LAS u32*)L(L_LIST + j * 8) > me.x;
                    if (rank < 8) *(LAS v2u*)L(L_SEL + rank * 8) = me;
                }
            }
            bar();
            STAMP(4);
            {
                const int s = tid / 72, ch = tid % 72;
                const v2u me = *(LAS v2u*)L(L_SEL + s * 8);
                const int tok = (2047 - me.x) & 2047;
                const v4u xr = ld128<0>(rq, tok * 1152 + ch * 16, 0);
                const float mb = lo16(me.y), inv = __builtin_amdgcn_rcpf(hi16(me.y));
                v4u o;
#pragma unroll
                for (int i = 0; i < 4; ++i) {
                    const f2 t = rbf2(unpk(xr[i]) - mb) * inv;
                    o[i] = nos(pk(t.x, t.y));
                }
                st128<SC1>(o, rw, tid * 16, W_XSEL);
                if (tid < 4) st128<SC1>(*(LAS v4u*)L(L_SEL + tid * 16), rw, tid * 16, W_SEL);
            }
            STAMP(5);
        } else {
            bar();
            if (isRed && !isK) {
                const int rho = ridx * 4 + lw, s = rho / 18, cb = rho % 18, d = 32 * cb + (lane & 31);
                cwait(C_SEL);
                STAMP(3);
                const v2u me = *(LAS v2u*)L(L_SEL + s * 8);
                const int tok = (2047 - me.x) & 2047;
                const u32 ord = me.x >> 16, rb = (ord & 0x8000u) ? (ord & 0x7FFFu) : (~ord & 0xFFFFu);
                const float mb = lo16(me.y), inv = __builtin_amdgcn_rcpf(hi16(me.y));
                const float x = lo16(p.q[tok * D + d]);
                float ks = 0.f;
#pragma unroll
                for (int j = 3; j >= 0; --j) ks += (tok - j >= 0 ? lo16(p.k[(tok - j) * D + d]) : 0.f) * lo16(p.sk[d * 4 + j]);
                const float kv = rbf(ks * lo16(p.v[tok * D + d]));
                const float xn = rbf(rbf(x - mb) * inv);
                const float kd = kv * lo16(p.dv[d]);
                const float rsg = rbf(__builtin_amdgcn_rcpf(1.0f + __expf(-lo16(rb))));
                cwait(C_F2);
                wait_word(rw, W_PART + 71 * 18432 + s * 2304 + cb * 128);
                STAMP(4);
                u32 vo[9];
                v4u pv[9];
#pragma unroll
                for (int i = 0; i < 9; ++i) vo[i] = (8 * i + (lane >> 3)) * 18432 + (lane & 7) * 16;
                poll16<9>(rw, vo, W_PART + s * 2304 + cb * 128, pv);
                f4 a = {0.f, 0.f, 0.f, 0.f};
#pragma unroll
                for (int i = 0; i < 9; ++i) a += __builtin_bit_cast(f4, pv[i]);
#pragma unroll
                for (int i = 0; i < 4; ++i) {
                    float t = a[i];
                    t += asf(dpp<0x128>(asu(t)));
                    const auto r1 = __builtin_amdgcn_permlane16_swap(asu(t), asu(t), false, false);
                    t = asf(r1[0]) + asf(r1[1]);
                    const auto r2 = __builtin_amdgcn_permlane32_swap(asu(t), asu(t), false, false);
                    a[i] = asf(r2[0]) + asf(r2[1]);
                }
                if (lane < 8) *(LAS f4*)L(L_RED + lw * 128 + lane * 16) = a;
                asm volatile("s_waitcnt lgkmcnt(0)" ::: "memory");
                const float moe = rbf(rbf(*(LAS float*)L(L_RED + lw * 128 + (lane & 31) * 4)) * rsg);
                STAMP(5);
                const int c = (d & 1) * 288 + (d >> 1);
                const u32 yo = (c >> 2) * 16384 + (c & 3) * 16 + (tok >> 3) * 64 + ((tok & 7) >> 1) * 4;
                u32 yw = SENT;
                for (u32 n = 0; n < SPIN_CAP; ++n) {
                    FENCE();
                    yw = ld32<SC1>(rw, yo, W_Y);
                    if (__all(yw != SENT)) break;
                    __builtin_amdgcn_s_sleep(2);
                }
                const float y = (tok & 1) ? hi16(yw) : lo16(yw);
                const float o = x + xn * (y + kd) + rbf(xn + moe);
                if (lane < 32) p.out[tok * D + d] = (u16)pk(o, 0.f);
                STAMP(6);
            }
        }

        if (isK) {
            f2 xn[8], pb[8];
            const v4u qa = *(LAS v4u*)L(L_ST + tid * 80), qb = *(LAS v4u*)L(L_ST + tid * 80 + 16);
            const u32 qq[8] = {qa.x, qa.y, qa.z, qa.w, qb.x, qb.y, qb.z, qb.w};
            const v4u kvc[2] = {*(LAS v4u*)L(L_ST + tid * 80 + 32), *(LAS v4u*)L(L_ST + tid * 80 + 48)};
            const f2 dv = unpk(*(LAS u32*)L(L_ST + tid * 80 + 64));
#pragma unroll
            for (int t = 0; t < 8; ++t) {
                const f2 st = *(LAS f2*)L(L_STAT + t * 8);
                const f2 x = unpk(qq[t]);
                xn[t] = rbf2(rbf2(x - st.x) * st.y);
                const u32 c0 = kvc[0][(7 - t) >> 1], c1 = kvc[1][(7 - t) >> 1];
                const f2 kv = (t & 1) ? f2{lo16(c0), lo16(c1)} : f2{hi16(c0), hi16(c1)};
                pb[t] = x + xn[t] + xn[t] * (kv * dv);
            }
#pragma unroll
            for (int t = 0; t < 8; ++t) asm volatile("" : "+v"(xn[t]), "+v"(pb[t]));
            STAMP(8);
            if (isConv) {
                bar();
                if (Y_DELAY) __builtin_amdgcn_s_sleep(Y_DELAY);
            } else {
                cwait(C_F2);
                wait_word(rw, W_Y + (18 * xq + 17) * 16384 + b * 64);
            }
            STAMP(9);
            if (tid < 288) {
                u32 vo[2] = {(u32)((cp >> 2) * 16384 + (cp & 3) * 16), (u32)(((cp + 288) >> 2) * 16384 + (cp & 3) * 16)};
                v4u yv[2];
                poll16<2>(rw, vo, W_Y + b * 64, yv);
                STAMP(10);
                u32 o[8];
#pragma unroll
                for (int t = 0; t < 8; ++t) {
                    const u32 c0 = yv[0][t >> 1], c1 = yv[1][t >> 1];
                    const f2 y = (t & 1) ? f2{hi16(c0), hi16(c1)} : f2{lo16(c0), lo16(c1)};
                    const f2 r = pb[t] + xn[t] * y;
                    o[t] = pk(r.x, r.y);
                }
                cwait(C_SEL);
                u32 mask = 0;
                {
                    const v4u s0 = *(LAS v4u*)L(L_SEL), s1 = *(LAS v4u*)L(L_SEL + 16), s2 = *(LAS v4u*)L(L_SEL + 32), s3 = *(LAS v4u*)L(L_SEL + 48);
                    const u32 ky[8] = {s0.x, s0.z, s1.x, s1.z, s2.x, s2.z, s3.x, s3.z};
#pragma unroll
                    for (int i = 0; i < 8; ++i) {
                        const u32 rel = (2047u - (ky[i] & 0xFFFFu)) - 8u * b;
                        mask |= rel < 8u ? 1u << rel : 0u;
                    }
                    mask = __builtin_amdgcn_readfirstlane(mask);
                }
                const rsrc_t ro = mkrs(p.out, S * D * 2);
#pragma unroll
                for (int t = 0; t < 8; ++t)
                    if (!((mask >> t) & 1u)) st32<AUX_OUT>(o[t], ro, tid * 4, (8 * b + t) * 1152);
                STAMP(11);
            }
        }
    }
}

extern "C" void kernel_launch(void* const* d_in, const int* in_sizes, int n_in, void* d_out, int out_size, void* d_ws, size_t ws_size,
                              hipStream_t stream) {
    Params p;
    p.q = (const u16*)d_in[0];
    p.k = (const u16*)d_in[1];
    p.v = (const u16*)d_in[2];
    p.sk = (const u16*)d_in[3];
    p.h = (const u16*)d_in[4];
    p.dv = (const u16*)d_in[5];
    p.wr = (const u16*)d_in[7];
    p.wg = (const u16*)d_in[8];
    p.wu = (const u16*)d_in[9];
    p.wd = (const u16*)d_in[10];
    p.out = (u16*)d_out;
    p.ws = (char*)d_ws + (WS_AT_END ? ((ws_size - WS_BYTES) & ~(size_t)4095) : 0);
    fused_kernel<<<256, NT, 0, stream>>>(p);
}
```

```cpp
#define PADUS 40
#define AUX_OUT 16
#include <hip/hip_runtime.h>
#include <stdint.h>

typedef unsigned short u16;
typedef unsigned u32;
typedef unsigned long long u64;
typedef u32 v4u __attribute__((ext_vector_type(4)));
typedef u32 v2u __attribute__((ext_vector_type(2)));
typedef float f4 __attribute__((ext_vector_type(4)));
typedef float f2 __attribute__((ext_vector_type(2)));
typedef __bf16 bf8 __attribute__((ext_vector_type(8)));
typedef __bf16 bf2 __attribute__((ext_vector_type(2)));
#define GAS __attribute__((address_space(1)))
#define LAS __attribute__((address_space(3)))
#define RLX_AGENT __ATOMIC_RELAXED, __HIP_MEMORY_SCOPE_AGENT
typedef __amdgpu_buffer_rsrc_t rsrc_t;

#define AUX_IN 0
#define AUX_W 2
#define WS_AT_END 1
#define U_DELAY 0
#define Y_DELAY 0

constexpr int S = 2048, D = 576, F = 4608;
constexpr int NT = 640;
constexpr u32 SENT = 0xFFFFFFFFu;
constexpr int SC1 = 16;
constexpr u32 SPIN_CAP = 1u << 16;

constexpr int W_E = 0, W_F1 = 4096, W_F2 = W_F1 + 256 * 128, W_KEY = W_F2 + 256 * 128, W_SEL = W_KEY + 256 * 64, W_XSEL = W_SEL + 128,
              W_U = W_XSEL + 9216 + 896, W_Y = W_U + 256 * 9216, W_PART = W_Y + 144 * 16384, W_END = W_PART + 72 * 18432, W_DBG = W_END;
constexpr int WS_BYTES = W_END + (1 << 20);
static_assert(W_U % 128 == 0 && W_XSEL % 128 == 0, "align");
constexpr int HC = 4160, HCH = 4 * HC, L_H = 0, UCH = 290 * 16, L_U = L_H + 4 * HCH, L_Y = L_U + 4 * UCH, L_ST = L_Y + 16384, L_C = L_ST + 320 * 80,
              L_STAT = L_C + 64, L_SEL = L_STAT + 64, L_WM = L_SEL + 64, L_RED = L_WM + 64, L_END = L_RED + 4 * 128;
constexpr int L_R = 0, L_HA = 9 * 8 * 128 * 4, HAS = 144, L_XS = L_HA + 8 * HAS, XSS = 1168;
constexpr int L_LIST = 0;
enum { C_CLR = 0, C_F1 = 1, C_F2 = 2, C_SEL = 3, C_UST = 4, C_CNT = 5 };

struct Params {
    const u16 *q, *k, *v, *sk, *h, *dv, *wr, *wg, *wu, *wd;
    u16* out;
    char* ws;
};

__device__ __forceinline__ float asf(u32 x) { return __builtin_bit_cast(float, x); }
__device__ __forceinline__ u32 asu(float x) { return __builtin_bit_cast(u32, x); }
__device__ __forceinline__ float lo16(u32 x) { return asf(x << 16); }
__device__ __forceinline__ float hi16(u32 x) { return asf(x & 0xFFFF0000u); }
__device__ __forceinline__ f2 unpk(u32 x) { return f2{lo16(x), hi16(x)}; }
__device__ __forceinline__ u32 pk(float a, float b) { return __builtin_bit_cast(u32, __builtin_convertvector(f2{a, b}, bf2)); }
__device__ __forceinline__ float rbf(float a) { return lo16(pk(a, a)); }
__device__ __forceinline__ f2 rbf2(f2 a) { return unpk(pk(a.x, a.y)); }
__device__ __forceinline__ u32 nos(u32 x) { return x < 0xFFFFFFFEu ? x : 0xFFFFFFFEu; }
__device__ __forceinline__ bool valid4(v4u v) { return v.x != SENT && v.y != SENT && v.z != SENT && v.w != SENT; }
__device__ __forceinline__ rsrc_t mkrs(const void* p, u32 bytes) { return __builtin_amdgcn_make_buffer_rsrc((void*)p, 0, bytes, 0x00020000); }
template <int AUX> __device__ __forceinline__ u32 ld32(rsrc_t r, u32 vo, u32 so) { return __builtin_amdgcn_raw_buffer_load_b32(r, vo, so, AUX); }
template <int AUX> __device__ __forceinline__ v2u ld64(rsrc_t r, u32 vo, u32 so) { return __builtin_amdgcn_raw_buffer_load_b64(r, vo, so, AUX); }
template <int AUX> __device__ __forceinline__ v4u ld128(rsrc_t r, u32 vo, u32 so) { return __builtin_amdgcn_raw_buffer_load_b128(r, vo, so, AUX); }
template <int AUX> __device__ __forceinline__ void st32(u32 v, rsrc_t r, u32 vo, u32 so) { __builtin_amdgcn_raw_buffer_store_b32(v, r, vo, so, AUX); }
template <int AUX> __device__ __forceinline__ void st64(v2u v, rsrc_t r, u32 vo, u32 so) { __builtin_amdgcn_raw_buffer_store_b64(v, r, vo, so, AUX); }
template <int AUX> __device__ __forceinline__ void st128(v4u v, rsrc_t r, u32 vo, u32 so) { __builtin_amdgcn_raw_buffer_store_b128(v, r, vo + so, 0, AUX); }
#define FENCE() asm volatile("" ::: "memory")
#define DRAIN() asm volatile("s_waitcnt vmcnt(0)" ::: "memory")
__device__ __forceinline__ void bar() {
    __builtin_amdgcn_sched_barrier(0);
    asm volatile("s_waitcnt lgkmcnt(0)" ::: "memory");
    __builtin_amdgcn_s_barrier();
    FENCE();
    __builtin_amdgcn_sched_barrier(0);
}
template <int CTRL> __device__ __forceinline__ u32 dpp(u32 x) { return (u32)__builtin_amdgcn_update_dpp(0, (int)x, CTRL, 0xF, 0xF, true); }
__device__ __forceinline__ float sum32(float x) {
    x += asf(dpp<0xB1>(asu(x)));
    x += asf(dpp<0x4E>(asu(x)));
    x += asf(dpp<0x141>(asu(x)));
    x += asf(dpp<0x140>(asu(x)));
    auto r = __builtin_amdgcn_permlane16_swap(asu(x), asu(x), false, false);
    return asf(r[0]) + asf(r[1]);
}
__device__ __forceinline__ u32 umax(u32 a, u32 b) { return a > b ? a : b; }
__device__ __forceinline__ u32 umin(u32 a, u32 b) { return a < b ? a : b; }
__device__ __forceinline__ u32 max64(u32 x) {
    x = umax(x, dpp<0xB1>(x));
    x = umax(x, dpp<0x4E>(x));
    x = umax(x, dpp<0x141>(x));
    x = umax(x, dpp<0x140>(x));
    auto r = __builtin_amdgcn_permlane16_swap(x, x, false, false);
    x = umax(r[0], r[1]);
    auto s = __builtin_amdgcn_permlane32_swap(x, x, false, false);
    return umax(s[0], s[1]);
}
__device__ __forceinline__ f4 mfma(v4u a, v4u b, f4 c) {
    return __builtin_amdgcn_mfma_f32_16x16x32_bf16(__builtin_bit_cast(bf8, a), __builtin_bit_cast(bf8, b), c, 0, 0, 0);
}

#define STAMP(i) do { } while (0)

__shared__ __attribute__((aligned(16))) char lds_raw[L_END];

__device__ __forceinline__ LAS char* L(int off) { return (LAS char*)lds_raw + off; }
__device__ __forceinline__ void cset(int w) { *(volatile LAS u32*)L(L_C + 4 * w) = 1u; }
__device__ __forceinline__ void cwait(int w) {
    for (u32 n = 0; n < (1u << 22); ++n) {
        if (*(volatile LAS u32*)L(L_C + 4 * w)) break;
        __builtin_amdgcn_s_sleep(1);
    }
    FENCE();
}
__device__ __forceinline__ void wait_flags(rsrc_t rw, int base, u32 tlo, u32 thi, int lane) {
    for (u32 n = 0; n < SPIN_CAP; ++n) {
        FENCE();
        bool ok = true;
#pragma unroll
        for (int i = 0; i < 4; ++i) {
            const v2u f = ld64<SC1>(rw, (lane + 64 * i) * 128, base);
            ok &= f.x == tlo && f.y == thi;
        }
        if (__all(ok)) break;
        __builtin_amdgcn_s_sleep(4);
    }
    FENCE();
}
__device__ __forceinline__ void wait_word(rsrc_t rw, u32 off) {
    for (u32 n = 0; n < SPIN_CAP; ++n) {
        FENCE();
        if (ld32<SC1>(rw, 0, off) != SENT) break;
        __builtin_amdgcn_s_sleep(2);
    }
    FENCE();
}
template <int N> __device__ __forceinline__ void poll16(rsrc_t rw, const u32 (&vo)[N], u32 so, v4u (&v)[N]) {
    bool ok[N];
#pragma unroll
    for (int i = 0; i < N; ++i) ok[i] = false;
    for (u32 n = 0; n < SPIN_CAP; ++n) {
        FENCE();
#pragma unroll
        for (int i = 0; i < N; ++i) if (!ok[i]) v[i] = ld128<SC1>(rw, vo[i], so);
        bool all = true;
#pragma unroll
        for (int i = 0; i < N; ++i) { ok[i] = valid4(v[i]); all &= ok[i]; }
        if (__all(all)) break;
    }
    FENCE();
}

#define DSR64(dst, addr, off) asm volatile("ds_read_b64 %0, %1 offset:%2" : "=v"(dst) : "v"(addr), "i"(off))
#define DSR128(dst, addr, off) asm volatile("ds_read_b128 %0, %1 offset:%2" : "=v"(dst) : "v"(addr), "i"(off))
#define SB() __builtin_amdgcn_sched_barrier(0)
#define MFMA_IP(acc, a, b) asm volatile("v_mfma_f32_16x16x32_bf16 %0, %1, %2, %0" : "+v"(acc) : "v"(a), "v"(b))

struct AF { v2u lo, hi; };
__device__ __forceinline__ v4u cat(const AF& a) { return v4u{a.lo.x, a.lo.y, a.hi.x, a.hi.y}; }

template <int I0> __device__ __forceinline__ void conv_phase(u32 va, u32 vb, f4 (&acc)[8], AF (&alo)[4], v4u (&blo)[4], AF (&alo2)[4], v4u (&blo2)[4]) {
    AF ahi[4];
    v4u bhi[4];
    constexpr int OA = 64 * I0, OB = 448 - 64 * I0;
    asm volatile("s_waitcnt lgkmcnt(0)" : "+v"(alo[0].lo), "+v"(alo[0].hi), "+v"(alo[1].lo), "+v"(alo[1].hi), "+v"(alo[2].lo), "+v"(alo[2].hi), "+v"(alo[3].lo),
                 "+v"(alo[3].hi), "+v"(blo[0]), "+v"(blo[1]), "+v"(blo[2]), "+v"(blo[3]));
    SB();
#pragma unroll
    for (int n = 0; n < 16; ++n) {
        const int e = n >> 2, b = n & 3;
        MFMA_IP(acc[e + b], cat(alo[e]), blo[b]);
        if (n < 4) DSR128(bhi[n], vb, OB + 512 * (4 + n));
        else if (n < 12) {
            if (n & 1) DSR64(ahi[(n - 4) >> 1].hi, va, OA + 512 * (4 + ((n - 4) >> 1)) + 8);
            else DSR64(ahi[(n - 4) >> 1].lo, va, OA + 512 * (4 + ((n - 4) >> 1)));
        }
        SB();
    }
    asm volatile("s_waitcnt lgkmcnt(0)" : "+v"(ahi[0].lo), "+v"(ahi[0].hi), "+v"(ahi[1].lo), "+v"(ahi[1].hi), "+v"(ahi[2].lo), "+v"(ahi[2].hi), "+v"(ahi[3].lo),
                 "+v"(ahi[3].hi), "+v"(bhi[0]), "+v"(bhi[1]), "+v"(bhi[2]), "+v"(bhi[3]));
    SB();
    int n = 0;
#pragma unroll
    for (int half = 0; half < 2; ++half)
#pragma unroll
        for (int e = 0; e < 4; ++e)
#pragma unroll
            for (int b = 0; b < 4 - e; ++b) {
                if (half == 0) MFMA_IP(acc[4 + e + b], cat(alo[e]), bhi[b]);
                else MFMA_IP(acc[4 + e + b], cat(ahi[e]), blo[b]);
                if (I0 < 7) {
                    if (n < 4) DSR128(blo2[n], vb, OB - 64 + 512 * n);
                    else if (n < 12) {
                        if (n & 1) DSR64(alo2[(n - 4) >> 1].hi, va, OA + 64 + 512 * ((n - 4) >> 1) + 8);
                        else DSR64(alo2[(n - 4) >> 1].lo, va, OA + 64 + 512 * ((n - 4) >> 1));
                    }
                }
                ++n;
                SB();
            }
}

__global__ void __launch_bounds__(NT, 3) fused_kernel(Params p) {
    const int tid = threadIdx.x, lane = tid & 63, wave = __builtin_amdgcn_readfirstlane(tid >> 6);
    const int wg = blockIdx.x, xq = wg & 7, xi = wg >> 3, b = 32 * xq + xi;
    const bool isConv = xi < 18, isMoe = xi >= 18 && xi < 27;
    const int ridx = 5 * xq + xi - 27;
    const bool isRed = xi >= 27 && ridx < 36, isSel = xi >= 27 && ridx == 39;
    const int cj = 18 * xq + xi, mu = 9 * xq + xi - 18;
    constexpr int SELWG = 255;
    STAMP(0);
    const u64 pad0 = __builtin_amdgcn_s_memrealtime();
    asm volatile("" ::"s"(p.q), "s"(p.k), "s"(p.v), "s"(p.sk), "s"(p.h), "s"(p.dv), "s"(p.wr), "s"(p.wg), "s"(p.wu), "s"(p.wd), "s"(p.out), "s"(p.ws));
    const rsrc_t rw = mkrs(p.ws, WS_BYTES);
    const rsrc_t rq = mkrs(p.q, S * D * 2);

    if (wave == 9) {
        const v4u s4 = {SENT, SENT, SENT, SENT};
        const v2u e = ld64<SC1>(rw, 0, W_E);
#pragma unroll
        for (int i = 0; i < 9; ++i) st128<SC1>(s4, rw, lane * 16 + 1024 * i, W_U + b * 9216);
        if (lane < 4) st128<SC1>(s4, rw, lane * 16, W_KEY + b * 64);
        if (isSel) {
#pragma unroll
            for (int i = 0; i < 9; ++i) st128<SC1>(s4, rw, lane * 16 + 1024 * i, W_XSEL);
            if (lane < 4) st128<SC1>(s4, rw, lane * 16, W_SEL);
        }
        if (lane < 16) *(LAS u32*)L(L_C + 4 * lane) = 0u;
        bar();
        const u64 tag = (((u64)e.y << 32) | e.x) + 1ull;
        const u32 tlo = __builtin_amdgcn_readfirstlane((u32)tag), thi = __builtin_amdgcn_readfirstlane((u32)(tag >> 32));
        DRAIN();
        STAMP(1);
        if (lane == 0) st64<SC1>(v2u{tlo, thi}, rw, 0, W_F1 + wg * 128);
        cset(C_CLR);
        if (isConv) {
            wait_flags(rw, W_F1, tlo, thi, lane);
            cset(C_F1);
            STAMP(2);
#pragma unroll
            for (int i = 0; i < 16; ++i) st128<SC1>(s4, rw, lane * 16 + 1024 * i, W_Y + cj * 16384);
            bar();
            DRAIN();
            if (lane == 0) st64<SC1>(v2u{tlo, thi}, rw, 0, W_F2 + wg * 128);
            STAMP(3);
            wait_flags(rw, W_F2, tlo, thi, lane);
            cset(C_F2);
            STAMP(4);
            bar();
        } else if (isMoe) {
#pragma unroll
            for (int i = 0; i < 18; ++i) st128<SC1>(s4, rw, lane * 16 + 1024 * i, W_PART + mu * 18432);
            DRAIN();
            if (lane == 0) st64<SC1>(v2u{tlo, thi}, rw, 0, W_F2 + wg * 128);
            STAMP(2);
            for (u32 n = 0; n < SPIN_CAP; ++n) {
                FENCE();
                const v2u f = ld64<SC1>(rw, 0, W_F1 + SELWG * 128);
                if (f.x == tlo && f.y == thi) break;
                __builtin_amdgcn_s_sleep(4);
            }
            wait_word(rw, W_XSEL + 9212);
            STAMP(3);
            u32 vo[9];
            v4u xv[9];
#pragma unroll
            for (int i = 0; i < 9; ++i) vo[i] = (lane + 64 * i) * 16;
            poll16<9>(rw, vo, W_XSEL, xv);
#pragma unroll
            for (int i = 0; i < 9; ++i) {
                const int c = lane + 64 * i;
                *(LAS v4u*)L(L_XS + (c / 72) * XSS + (c % 72) * 16) = xv[i];
            }
            STAMP(4);
            bar();
            bar();
            bar();
            wait_flags(rw, W_F2, tlo, thi, lane);
            cset(C_F2);
        } else {
            if (lane == 0) st64<SC1>(v2u{tlo, thi}, rw, 0, W_F2 + wg * 128);
            if (isSel) {
                wait_flags(rw, W_F1, tlo, thi, lane);
                if (lane == 0) st64<SC1>(v2u{tlo, thi}, rw, 0, W_E);
                STAMP(2);
                bar();
                bar();
                bar();
                bar();
            } else {
                bar();
            }
            wait_flags(rw, W_F2, tlo, thi, lane);
            cset(C_F2);
            STAMP(4);
        }
        if (!isSel) {
            u32 vo[1] = {(u32)(lane & 3) * 16};
            v4u sv[1];
            poll16<1>(rw, vo, W_SEL, sv);
            if (lane < 4) *(LAS v4u*)L(L_SEL + lane * 16) = sv[0];
            asm volatile("s_waitcnt lgkmcnt(0)" ::: "memory");
        }
        cset(C_SEL);
        STAMP(5);
    } else {
        const bool isK = wave < 5;
        const int cp = tid;
        const int lw = wave - 5;
        v4u hd[4];
        v2u hn[4];
        if (isK) {
            const rsrc_t rk = mkrs(p.k, S * D * 2), rv = mkrs(p.v, S * D * 2);
            const u32 vo = tid < 288 ? tid * 4 : 0x40000000u;
            u32 kk[11], vv[8], qq[8];
            v4u kvc[2];
#pragma unroll
            for (int i = 0; i < 11; ++i) {
                const int row = 8 * b - 3 + i;
                kk[i] = ld32<AUX_IN>(rk, vo, row >= 0 ? row * 1152 : 0x40000000);
            }
#pragma unroll
            for (int i = 0; i < 8; ++i) vv[i] = ld32<AUX_IN>(rv, vo, (8 * b + i) * 1152);
            const v4u sk = ld128<0>(mkrs(p.sk, D * 8), tid * 16, 0);
#pragma unroll
            for (int i = 0; i < 8; ++i) qq[i] = ld32<AUX_IN>(rq, vo, (8 * b + i) * 1152);
            const u32 dvp = ld32<0>(mkrs(p.dv, D * 2), vo, 0);
            bar();
            const f2 w0 = {lo16(sk.x), lo16(sk.z)}, w1 = {hi16(sk.x), hi16(sk.z)}, w2 = {lo16(sk.y), lo16(sk.w)}, w3 = {hi16(sk.y), hi16(sk.w)};
            f2 kf[11], r[8];
#pragma unroll
            for (int i = 0; i < 11; ++i) kf[i] = unpk(kk[i]);
#pragma unroll
            for (int t = 0; t < 8; ++t) r[t] = (kf[t] * w3 + kf[t + 1] * w2 + kf[t + 2] * w1 + kf[t + 3] * w0) * unpk(vv[t]);
            kvc[0] = v4u{nos(pk(r[7].x, r[6].x)), nos(pk(r[5].x, r[4].x)), nos(pk(r[3].x, r[2].x)), nos(pk(r[1].x, r[0].x))};
            kvc[1] = v4u{nos(pk(r[7].y, r[6].y)), nos(pk(r[5].y, r[4].y)), nos(pk(r[3].y, r[2].y)), nos(pk(r[1].y, r[0].y))};
            STAMP(1);
            cwait(C_CLR);
            if (tid < 288) {
                st128<SC1>(kvc[0], rw, tid * 16, W_U + b * 9216);
                st128<SC1>(kvc[1], rw, tid * 16, W_U + b * 9216 + 4608);
            }
            cset(C_UST);
            STAMP(2);
            *(LAS v4u*)L(L_ST + tid * 80) = v4u{qq[0], qq[1], qq[2], qq[3]};
            *(LAS v4u*)L(L_ST + tid * 80 + 16) = v4u{qq[4], qq[5], qq[6], qq[7]};
            *(LAS v4u*)L(L_ST + tid * 80 + 32) = kvc[0];
            *(LAS v4u*)L(L_ST + tid * 80 + 48) = kvc[1];
            *(LAS u32*)L(L_ST + tid * 80 + 64) = dvp;
        } else {
            const int hf = lane >> 5, l32 = lane & 31, tl = 2 * lw + hf, tok = 8 * b + tl;
            const rsrc_t rr = mkrs(p.wr, D * 2);
            u32 xq9[9], wr9[9];
#pragma unroll
            for (int i = 0; i < 9; ++i) xq9[i] = ld32<AUX_IN>(rq, tok * 1152 + l32 * 4, 128 * i);
#pragma unroll
            for (int i = 0; i < 9; ++i) wr9[i] = ld32<0>(rr, l32 * 4, 128 * i);
            {
                const int c = 4 * cj + lw, d = c < 288 ? 2 * c : 2 * (c - 288) + 1;
                const rsrc_t rh = mkrs(p.h, isConv ? D * S * 2 : 0);
#pragma unroll
                for (int i = 0; i < 4; ++i) hd[i] = ld128<AUX_IN>(rh, lane * 16 + 1024 * i, d * 4096);
#pragma unroll
                for (int i = 0; i < 4; ++i) hn[i] = ld64<AUX_IN>(rh, lane * 16 + 1024 * i + 16, d * 4096);
            }
            bar();
            f2 x[9];
            f2 s2 = {0.f, 0.f};
#pragma unroll
            for (int i = 0; i < 9; ++i) { x[i] = unpk(xq9[i]); s2 += x[i]; }
            const float mean = sum32(s2.x + s2.y) * (1.0f / D);
            f2 v2 = {0.f, 0.f};
#pragma unroll
            for (int i = 0; i < 9; ++i) { const f2 dl = x[i] - mean; v2 += dl * dl; }
            const float var = rbf(sum32(v2.x + v2.y) * (1.0f / D));
            const float mb = rbf(mean), sd = rbf(__builtin_amdgcn_sqrtf(rbf(var + 1e-5f)));
            const float inv = __builtin_amdgcn_rcpf(sd);
            f2 a2 = {0.f, 0.f};
#pragma unroll
            for (int i = 0; i < 9; ++i) a2 += rbf2(rbf2(x[i] - mb) * inv) * unpk(wr9[i]);
            const float rs = sum32(a2.x + a2.y);
            const u32 rb = pk(rs, rs) & 0xFFFFu;
            u32 ord = (rb & 0x8000u) ? (~rb & 0xFFFFu) : (rb | 0x8000u);
            if ((rb & 0x7FFFu) > 0x7F80u) ord = 0u;
            const v2u rec = {(ord << 16) | (u32)(2047 - tok), nos(pk(mb, sd))};
            STAMP(1);
            cwait(C_CLR);
            if (l32 == 0) {
                st64<SC1>(rec, rw, tl * 8, W_KEY + b * 64);
                *(LAS f2*)L(L_STAT + tl * 8) = f2{mb, inv};
            }
            STAMP(2);
        }

        if (isConv) {
            if (isK) {
                bar();
            } else {
                LAS char* hb = L(L_H + lw * HCH);
#pragma unroll
                for (int i = 0; i < 4; ++i) {
                    const v4u dd = hd[i];
                    const v2u nn = hn[i];
                    const u32 a01 = __builtin_amdgcn_alignbit(dd.y, dd.x, 16), a12 = __builtin_amdgcn_alignbit(dd.z, dd.y, 16),
                              a23 = __builtin_amdgcn_alignbit(dd.w, dd.z, 16), a3n = __builtin_amdgcn_alignbit(nn.x, dd.w, 16),
                              ann = __builtin_amdgcn_alignbit(nn.y, nn.x, 16);
                    LAS char* o = hb + (lane + 64 * i + 2) * 16;
                    *(LAS v4u*)(o) = dd;
                    *(LAS v4u*)(o + HC) = v4u{a01, a12, a23, a3n};
                    *(LAS v4u*)(o + 2 * HC) = v4u{dd.y, dd.z, dd.w, nn.x};
                    *(LAS v4u*)(o + 3 * HC) = v4u{a12, a23, a3n, ann};
                }
                {
                    const u32 h0 = __builtin_amdgcn_readfirstlane(hd[0].x), h1 = __builtin_amdgcn_readfirstlane(hd[0].y);
                    if (lane < 8) {
                        const int sg = lane >> 1;
                        v4u z = {0u, 0u, 0u, 0u};
                        if (lane & 1) {
                            if (sg == 1) z.w = h0 << 16;
                            else if (sg == 2) z.w = h0;
                            else if (sg == 3) { z.z = h0 << 16; z.w = (h0 >> 16) | (h1 << 16); }
                        }
                        *(LAS v4u*)(hb + sg * HC + (lane & 1) * 16) = z;
                    }
                    if (lane < 32) *(LAS v4u*)L(L_U + lw * UCH + lane * 16) = v4u{0u, 0u, 0u, 0u};
                }
                f4 acc[8];
#pragma unroll
                for (int i = 0; i < 8; ++i) {
                    acc[i] = f4{0.f, 0.f, 0.f, 0.f};
                    asm volatile("" : "+v"(acc[i]));
                }
                STAMP(3);
                cwait(C_F1);
                cwait(C_UST);
                if (U_DELAY) __builtin_amdgcn_s_sleep(U_DELAY);
                STAMP(4);
                {
                    const int l = tid - 320;
                    u32 vo[4];
                    v4u uv[4];
#pragma unroll
                    for (int i = 0; i < 4; ++i) vo[i] = (64 * i + (l >> 2)) * 9216 + (l & 3) * 16;
                    poll16<4>(rw, vo, W_U + cj * 64, uv);
#pragma unroll
                    for (int i = 0; i < 4; ++i) *(LAS v4u*)L(L_U + (l & 3) * UCH + (32 + 64 * i + (l >> 2)) * 16) = uv[i];
                }
                STAMP(5);
                bar();
                const int m1 = (lane & 15) + 1, kq = lane >> 4;
                const u32 va = (u32)(uintptr_t)(hb + (m1 & 3) * HC + 16 * kq + 8 * (m1 >> 2));
                const u32 vb = (u32)(uintptr_t)L(L_U + lw * UCH + (33 + 2 * (lane & 15) - kq) * 16 - 448);
                AF a0[4], a1[4];
                v4u b0[4], b1[4];
#pragma unroll
                for (int n = 0; n < 4; ++n) {
                    DSR128(b0[n], vb, 448 + 512 * n);
                    DSR64(a0[n].lo, va, 512 * n);
                    DSR64(a0[n].hi, va, 512 * n + 8);
                }
                conv_phase<0>(va, vb, acc, a0, b0, a1, b1);
                conv_phase<1>(va, vb, acc, a1, b1, a0, b0);
                conv_phase<2>(va, vb, acc, a0, b0, a1, b1);
                conv_phase<3>(va, vb, acc, a1, b1, a0, b0);
                conv_phase<4>(va, vb, acc, a0, b0, a1, b1);
                conv_phase<5>(va, vb, acc, a1, b1, a0, b0);
                conv_phase<6>(va, vb, acc, a0, b0, a1, b1);
                conv_phase<7>(va, vb, acc, a1, b1, a0, b0);
                asm volatile("s_nop 7\n\ts_nop 1" : "+v"(acc[0]), "+v"(acc[1]), "+v"(acc[2]), "+v"(acc[3]), "+v"(acc[4]), "+v"(acc[5]), "+v"(acc[6]), "+v"(acc[7]));
                STAMP(6);
#pragma unroll
                for (int q = 0; q < 8; q += 2) {
                    const u32 y0 = nos(pk(acc[q][0], acc[q][1])), y1 = nos(pk(acc[q][2], acc[q][3]));
                    const u32 z0 = nos(pk(acc[q + 1][0], acc[q + 1][1])), z1 = nos(pk(acc[q + 1][2], acc[q + 1][3]));
                    const auto s0 = __builtin_amdgcn_permlane16_swap(y0, z0, false, false);
                    const auto s1 = __builtin_amdgcn_permlane16_swap(y1, z1, false, false);
                    const int blk = 32 * (q + (kq & 1)) + 2 * (lane & 15) + (kq >> 1);
                    *(LAS v4u*)L(L_Y + blk * 64 + ((lw ^ (blk >> 1)) & 3) * 16) = v4u{s0[0], s1[0], s0[1], s1[1]};
                }
                bar();
                {
                    const int l = tid - 320;
#pragma unroll
                    for (int i = 0; i < 4; ++i) {
                        const int c = l + 256 * i, blk = c >> 2;
                        const v4u yv = *(LAS v4u*)L(L_Y + blk * 64 + (((c & 3) ^ (blk >> 1)) & 3) * 16);
                        st128<SC1>(yv, rw, c * 16, W_Y + cj * 16384);
                    }
                }
                STAMP(7);
            }
        } else if (isMoe) {
            const int c = lane & 15, kq = lane >> 4;
            const GAS char* gp = (const GAS char*)(c < 8 ? p.wg : p.wu) + (size_t)(64 * wave + 8 * kq) * (F * 2) + mu * 128 + (c & 7) * 16;
            v4u gr[2][8];
            v2u dr[2][8];
#pragma unroll
            for (int ks = 0; ks < 2; ++ks)
#pragma unroll
                for (int e = 0; e < 8; ++e) {
                    const GAS v4u* a = (const GAS v4u*)(gp + (size_t)(32 * ks + e) * (F * 2));
                    gr[ks][e] = AUX_W ? __builtin_nontemporal_load(a) : *a;
                }
            const rsrc_t rd = mkrs(p.wd, F * D * 2);
#pragma unroll
            for (int ks = 0; ks < 2; ++ks)
#pragma unroll
                for (int e = 0; e < 8; ++e) dr[ks][e] = ld64<AUX_W>(rd, kq * (8 * 1152) + c * 8, (64 * mu + 32 * ks + e) * 1152 + wave * 128);
            STAMP(3);
            v4u gf[2][8];
#pragma unroll
            for (int ks = 0; ks < 2; ++ks)
#pragma unroll
                for (int nn = 0; nn < 8; ++nn)
#pragma unroll
                    for (int pp = 0; pp < 4; ++pp)
                        gf[ks][nn][pp] = __builtin_amdgcn_perm(gr[ks][2 * pp + 1][nn >> 1], gr[ks][2 * pp][nn >> 1], (nn & 1) ? 0x07060302u : 0x05040100u);
#pragma unroll
            for (int ks = 0; ks < 2; ++ks)
#pragma unroll
                for (int nn = 0; nn < 8; ++nn) asm volatile("" : "+v"(gf[ks][nn]));
            STAMP(4);
            bar();
            STAMP(5);
            {
                v4u xa[2];
#pragma unroll
                for (int ks = 0; ks < 2; ++ks) {
                    xa[ks] = v4u{0u, 0u, 0u, 0u};
                    if (c < 8) xa[ks] = *(LAS v4u*)L(L_XS + c * XSS + (64 * wave + 32 * ks + 8 * kq) * 2);
                }
                f4 g[8];
#pragma unroll
                for (int nn = 0; nn < 8; ++nn) g[nn] = mfma(xa[1], gf[1][nn], mfma(xa[0], gf[0][nn], f4{0.f, 0.f, 0.f, 0.f}));
                if (kq < 2) {
#pragma unroll
                    for (int r = 0; r < 4; ++r) {
                        LAS char* o = L(L_R + ((wave * 8 + 4 * kq + r) * 128 + 8 * c) * 4);
                        *(LAS f4*)o = f4{g[0][r], g[1][r], g[2][r], g[3][r]};
                        *(LAS f4*)(o + 16) = f4{g[4][r], g[5][r], g[6][r], g[7][r]};
                    }
                }
            }
            bar();
            if (wave < 8) {
                float g = 0.f, u = 0.f;
#pragma unroll
                for (int w = 0; w < 9; ++w) {
                    g += *(LAS float*)L(L_R + ((w * 8 + wave) * 128 + lane) * 4);
                    u += *(LAS float*)L(L_R + ((w * 8 + wave) * 128 + 64 + lane) * 4);
                }
                g = rbf(g);
                u = rbf(u);
                const float sg = rbf(g * __builtin_amdgcn_rcpf(1.0f + __expf(-g)));
                *(LAS u16*)L(L_HA + wave * HAS + lane * 2) = (u16)pk(sg * u, 0.f);
            }
            v4u df[2][4];
#pragma unroll
            for (int ks = 0; ks < 2; ++ks)
#pragma unroll
                for (int nn = 0; nn < 4; ++nn)
#pragma unroll
                    for (int pp = 0; pp < 4; ++pp)
                        df[ks][nn][pp] = __builtin_amdgcn_perm(dr[ks][2 * pp + 1][nn >> 1], dr[ks][2 * pp][nn >> 1], (nn & 1) ? 0x07060302u : 0x05040100u);
            bar();
            STAMP(6);
            {
                v4u ha[2];
#pragma unroll
                for (int ks = 0; ks < 2; ++ks) {
                    ha[ks] = v4u{0u, 0u, 0u, 0u};
                    if (c < 8) ha[ks] = *(LAS v4u*)L(L_HA + c * HAS + (32 * ks + 8 * kq) * 2);
                }
                f4 y[4];
#pragma unroll
                for (int nn = 0; nn < 4; ++nn) y[nn] = mfma(ha[1], df[1][nn], mfma(ha[0], df[0][nn], f4{0.f, 0.f, 0.f, 0.f}));
                if (kq < 2) {
#pragma unroll
                    for (int r = 0; r < 4; ++r)
                        st128<SC1>(v4u{nos(asu(y[0][r])), nos(asu(y[1][r])), nos(asu(y[2][r])), nos(asu(y[3][r]))}, rw, (4 * kq + r) * 2304 + c * 16,
                                   W_PART + mu * 18432 + wave * 256);
                }
            }
            STAMP(7);
        } else if (isSel) {
            if (tid == 0) *(LAS u32*)L(L_C + 4 * C_CNT) = 0u;
            bar();
            const int sw = wave < 4 ? wave : wave - 1;
            v4u kv2[2];
            if (wave != 4) {
                u32 vo[2] = {(u32)(sw * 64 + lane) * 16, (u32)(sw * 64 + lane) * 16 + 8192};
                poll16<2>(rw, vo, W_KEY, kv2);
                const u32 mx = max64(umax(umax(kv2[0].x, kv2[0].z), umax(kv2[1].x, kv2[1].z)));
                if (lane == 0) *(LAS u32*)L(L_WM + 4 * sw) = mx;
            }
            STAMP(3);
            bar();
            if (wave != 4) {
                const v4u m0 = *(LAS v4u*)L(L_WM), m1 = *(LAS v4u*)L(L_WM + 16);
                const u32 thr = umin(umin(umin(m0.x, m0.y), umin(m0.z, m0.w)), umin(umin(m1.x, m1.y), umin(m1.z, m1.w)));
#pragma unroll
                for (int i = 0; i < 4; ++i) {
                    const u32 key = (i & 1) ? kv2[i >> 1].z : kv2[i >> 1].x, ms = (i & 1) ? kv2[i >> 1].w : kv2[i >> 1].y;
                    if (key >= thr) {
                        const u32 pos = __hip_atomic_fetch_add((LAS u32*)L(L_C + 4 * C_CNT), 1u, __ATOMIC_RELAXED, __HIP_MEMORY_SCOPE_WORKGROUP);
                        *(LAS v2u*)L(L_LIST + pos * 8) = v2u{key, ms};
                    }
                }
            }
            bar();
            {
                const int n = *(LAS u32*)L(L_C + 4 * C_CNT);
                for (int i = tid; i < n; i += 576) {
                    const v2u me = *(LAS v2u*)L(L_LIST + i * 8);
                    int rank = 0;
                    for (int j = 0; j < n; ++j) rank += *(LAS u32*)L(L_LIST + j * 8) > me.x;
                    if (rank < 8) *(LAS v2u*)L(L_SEL + rank * 8) = me;
                }
            }
            bar();
            STAMP(4);
            {
                const int s = tid / 72, ch = tid % 72;
                const v2u me = *(LAS v2u*)L(L_SEL + s * 8);
                const int tok = (2047 - me.x) & 2047;
                const v4u xr = ld128<0>(rq, tok * 1152 + ch * 16, 0);
                const float mb = lo16(me.y), inv = __builtin_amdgcn_rcpf(hi16(me.y));
                v4u o;
#pragma unroll
                for (int i = 0; i < 4; ++i) {
                    const f2 t = rbf2(unpk(xr[i]) - mb) * inv;
                    o[i] = nos(pk(t.x, t.y));
                }
                st128<SC1>(o, rw, tid * 16, W_XSEL);
                if (tid < 4) st128<SC1>(*(LAS v4u*)L(L_SEL + tid * 16), rw, tid * 16, W_SEL);
            }
            STAMP(5);
        } else {
            bar();
            if (isRed && !isK) {
                const int rho = ridx * 4 + lw, s = rho / 18, cb = rho % 18, d = 32 * cb + (lane & 31);
                cwait(C_SEL);
                STAMP(3);
                const v2u me = *(LAS v2u*)L(L_SEL + s * 8);
                const int tok = (2047 - me.x) & 2047;
                const u32 ord = me.x >> 16, rb = (ord & 0x8000u) ? (ord & 0x7FFFu) : (~ord & 0xFFFFu);
                const float mb = lo16(me.y), inv = __builtin_amdgcn_rcpf(hi16(me.y));
                const float x = lo16(p.q[tok * D + d]);
                float ks = 0.f;
#pragma unroll
                for (int j = 3; j >= 0; --j) ks += (tok - j >= 0 ? lo16(p.k[(tok - j) * D + d]) : 0.f) * lo16(p.sk[d * 4 + j]);
                const float kv = rbf(ks * lo16(p.v[tok * D + d]));
                const float xn = rbf(rbf(x - mb) * inv);
                const float kd = kv * lo16(p.dv[d]);
                const float rsg = rbf(__builtin_amdgcn_rcpf(1.0f + __expf(-lo16(rb))));
                cwait(C_F2);
                wait_word(rw, W_PART + 71 * 18432 + s * 2304 + cb * 128);
                STAMP(4);
                u32 vo[9];
                v4u pv[9];
#pragma unroll
                for (int i = 0; i < 9; ++i) vo[i] = (8 * i + (lane >> 3)) * 18432 + (lane & 7) * 16;
                poll16<9>(rw, vo, W_PART + s * 2304 + cb * 128, pv);
                f4 a = {0.f, 0.f, 0.f, 0.f};
#pragma unroll
                for (int i = 0; i < 9; ++i) a += __builtin_bit_cast(f4, pv[i]);
#pragma unroll
                for (int i = 0; i < 4; ++i) {
                    float t = a[i];
                    t += asf(dpp<0x128>(asu(t)));
                    const auto r1 = __builtin_amdgcn_permlane16_swap(asu(t), asu(t), false, false);
                    t = asf(r1[0]) + asf(r1[1]);
                    const auto r2 = __builtin_amdgcn_permlane32_swap(asu(t), asu(t), false, false);
                    a[i] = asf(r2[0]) + asf(r2[1]);
                }
                if (lane < 8) *(LAS f4*)L(L_RED + lw * 128 + lane * 16) = a;
                asm volatile("s_waitcnt lgkmcnt(0)" ::: "memory");
                const float moe = rbf(rbf(*(LAS float*)L(L_RED + lw * 128 + (lane & 31) * 4)) * rsg);
                STAMP(5);
                const int c = (d & 1) * 288 + (d >> 1);
                const u32 yo = (c >> 2) * 16384 + (c & 3) * 16 + (tok >> 3) * 64 + ((tok & 7) >> 1) * 4;
                u32 yw = SENT;
                for (u32 n = 0; n < SPIN_CAP; ++n) {
                    FENCE();
                    yw = ld32<SC1>(rw, yo, W_Y);
                    if (__all(yw != SENT)) break;
                    __builtin_amdgcn_s_sleep(2);
                }
                const float y = (tok & 1) ? hi16(yw) : lo16(yw);
                const float o = x + xn * (y + kd) + rbf(xn + moe);
                if (lane < 32) p.out[tok * D + d] = (u16)pk(o, 0.f);
                STAMP(6);
            }
        }

        if (isK) {
            f2 xn[8], pb[8];
            const v4u qa = *(LAS v4u*)L(L_ST + tid * 80), qb = *(LAS v4u*)L(L_ST + tid * 80 + 16);
            const u32 qq[8] = {qa.x, qa.y, qa.z, qa.w, qb.x, qb.y, qb.z, qb.w};
            const v4u kvc[2] = {*(LAS v4u*)L(L_ST + tid * 80 + 32), *(LAS v4u*)L(L_ST + tid * 80 + 48)};
            const f2 dv = unpk(*(LAS u32*)L(L_ST + tid * 80 + 64));
#pragma unroll
            for (int t = 0; t < 8; ++t) {
                const f2 st = *(LAS f2*)L(L_STAT + t * 8);
                const f2 x = unpk(qq[t]);
                xn[t] = rbf2(rbf2(x - st.x) * st.y);
                const u32 c0 = kvc[0][(7 - t) >> 1], c1 = kvc[1][(7 - t) >> 1];
                const f2 kv = (t & 1) ? f2{lo16(c0), lo16(c1)} : f2{hi16(c0), hi16(c1)};
                pb[t] = x + xn[t] + xn[t] * (kv * dv);
            }
#pragma unroll
            for (int t = 0; t < 8; ++t) asm volatile("" : "+v"(xn[t]), "+v"(pb[t]));
            STAMP(8);
            if (isConv) {
                bar();
                if (Y_DELAY) __builtin_amdgcn_s_sleep(Y_DELAY);
            } else {
                cwait(C_F2);
                wait_word(rw, W_Y + (18 * xq + 17) * 16384 + b * 64);
            }
            STAMP(9);
            if (tid < 288) {
                u32 vo[2] = {(u32)((cp >> 2) * 16384 + (cp & 3) * 16), (u32)(((cp + 288) >> 2) * 16384 + (cp & 3) * 16)};
                v4u yv[2];
                poll16<2>(rw, vo, W_Y + b * 64, yv);
                STAMP(10);
                u32 o[8];
#pragma unroll
                for (int t = 0; t < 8; ++t) {
                    const u32 c0 = yv[0][t >> 1], c1 = yv[1][t >> 1];
                    const f2 y = (t & 1) ? f2{hi16(c0), hi16(c1)} : f2{lo16(c0), lo16(c1)};
                    const f2 r = pb[t] + xn[t] * y;
                    o[t] = pk(r.x, r.y);
                }
                cwait(C_SEL);
                u32 mask = 0;
                {
                    const v4u s0 = *(LAS v4u*)L(L_SEL), s1 = *(LAS v4u*)L(L_SEL + 16), s2 = *(LAS v4u*)L(L_SEL + 32), s3 = *(LAS v4u*)L(L_SEL + 48);
                    const u32 ky[8] = {s0.x, s0.z, s1.x, s1.z, s2.x, s2.z, s3.x, s3.z};
#pragma unroll
                    for (int i = 0; i < 8; ++i) {
                        const u32 rel = (2047u - (ky[i] & 0xFFFFu)) - 8u * b;
                        mask |= rel < 8u ? 1u << rel : 0u;
                    }
                    mask = __builtin_amdgcn_readfirstlane(mask);
                }
                const rsrc_t ro = mkrs(p.out, S * D * 2);
#pragma unroll
                for (int t = 0; t < 8; ++t)
                    if (!((mask >> t) & 1u)) st32<AUX_OUT>(o[t], ro, tid * 4, (8 * b + t) * 1152);
                STAMP(11);
            }
        }
    }
    if (wg == 255) while (__builtin_amdgcn_s_memrealtime() - pad0 < PADUS * 100) __builtin_amdgcn_s_sleep(1);
}

extern "C" void kernel_launch(void* const* d_in, const int* in_sizes, int n_in, void* d_out, int out_size, void* d_ws, size_t ws_size,
                              hipStream_t stream) {
    Params p;
    p.q = (const u16*)d_in[0];
    p.k = (const u16*)d_in[1];
    p.v = (const u16*)d_in[2];
    p.sk = (const u16*)d_in[3];
    p.h = (const u16*)d_in[4];
    p.dv = (const u16*)d_in[5];
    p.wr = (const u16*)d_in[7];
    p.wg = (const u16*)d_in[8];
    p.wu = (const u16*)d_in[9];
    p.wd = (const u16*)d_in[10];
    p.out = (u16*)d_out;
    p.ws = (char*)d_ws + (WS_AT_END ? ((ws_size - WS_BYTES) & ~(size_t)4095) : 0);
    fused_kernel<<<256, NT, 0, stream>>>(p);
}
```

```cpp
#define PADUS 40
#define AUX_OUT 16
#define PROBE_A 0
#define PROBE_B 1
#define PROBE_COND (wave < 5)
#include <hip/hip_runtime.h>
#include <stdint.h>

typedef unsigned short u16;
typedef unsigned u32;
typedef unsigned long long u64;
typedef u32 v4u __attribute__((ext_vector_type(4)));
typedef u32 v2u __attribute__((ext_vector_type(2)));
typedef float f4 __attribute__((ext_vector_type(4)));
typedef float f2 __attribute__((ext_vector_type(2)));
typedef __bf16 bf8 __attribute__((ext_vector_type(8)));
typedef __bf16 bf2 __attribute__((ext_vector_type(2)));
#define GAS __attribute__((address_space(1)))
#define LAS __attribute__((address_space(3)))
#define RLX_AGENT __ATOMIC_RELAXED, __HIP_MEMORY_SCOPE_AGENT
typedef __amdgpu_buffer_rsrc_t rsrc_t;

#define AUX_IN 0
#define AUX_W 2
#define WS_AT_END 1
#define U_DELAY 0
#define Y_DELAY 0

constexpr int S = 2048, D = 576, F = 4608;
constexpr int NT = 640;
constexpr u32 SENT = 0xFFFFFFFFu;
constexpr int SC1 = 16;
constexpr u32 SPIN_CAP = 1u << 16;

constexpr int W_E = 0, W_F1 = 4096, W_F2 = W_F1 + 256 * 128, W_KEY = W_F2 + 256 * 128, W_SEL = W_KEY + 256 * 64, W_XSEL = W_SEL + 128,
              W_U = W_XSEL + 9216 + 896, W_Y = W_U + 256 * 9216, W_PART = W_Y + 144 * 16384, W_END = W_PART + 72 * 18432, W_DBG = W_END;
constexpr int WS_BYTES = W_END + (1 << 20);
static_assert(W_U % 128 == 0 && W_XSEL % 128 == 0, "align");
constexpr int HC = 4160, HCH = 4 * HC, L_H = 0, UCH = 290 * 16, L_U = L_H + 4 * HCH, L_Y = L_U + 4 * UCH, L_ST = L_Y + 16384, L_C = L_ST + 320 * 80,
              L_STAT = L_C + 64, L_SEL = L_STAT + 64, L_WM = L_SEL + 64, L_RED = L_WM + 64, L_END = L_RED + 4 * 128;
constexpr int L_R = 0, L_HA = 9 * 8 * 128 * 4, HAS = 144, L_XS = L_HA + 8 * HAS, XSS = 1168;
constexpr int L_LIST = 0;
enum { C_CLR = 0, C_F1 = 1, C_F2 = 2, C_SEL = 3, C_UST = 4, C_CNT = 5 };

struct Params {
    const u16 *q, *k, *v, *sk, *h, *dv, *wr, *wg, *wu, *wd;
    u16* out;
    char* ws;
};

__device__ __forceinline__ float asf(u32 x) { return __builtin_bit_cast(float, x); }
__device__ __forceinline__ u32 asu(float x) { return __builtin_bit_cast(u32, x); }
__device__ __forceinline__ float lo16(u32 x) { return asf(x << 16); }
__device__ __forceinline__ float hi16(u32 x) { return asf(x & 0xFFFF0000u); }
__device__ __forceinline__ f2 unpk(u32 x) { return f2{lo16(x), hi16(x)}; }
__device__ __forceinline__ u32 pk(float a, float b) { return __builtin_bit_cast(u32, __builtin_convertvector(f2{a, b}, bf2)); }
__device__ __forceinline__ float rbf(float a) { return lo16(pk(a, a)); }
__device__ __forceinline__ f2 rbf2(f2 a) { return unpk(pk(a.x, a.y)); }
__device__ __forceinline__ u32 nos(u32 x) { return x < 0xFFFFFFFEu ? x : 0xFFFFFFFEu; }
__device__ __forceinline__ bool valid4(v4u v) { return v.x != SENT && v.y != SENT && v.z != SENT && v.w != SENT; }
__device__ __forceinline__ rsrc_t mkrs(const void* p, u32 bytes) { return __builtin_amdgcn_make_buffer_rsrc((void*)p, 0, bytes, 0x00020000); }
template <int AUX> __device__ __forceinline__ u32 ld32(rsrc_t r, u32 vo, u32 so) { return __builtin_amdgcn_raw_buffer_load_b32(r, vo, so, AUX); }
template <int AUX> __device__ __forceinline__ v2u ld64(rsrc_t r, u32 vo, u32 so) { return __builtin_amdgcn_raw_buffer_load_b64(r, vo, so, AUX); }
template <int AUX> __device__ __forceinline__ v4u ld128(rsrc_t r, u32 vo, u32 so) { return __builtin_amdgcn_raw_buffer_load_b128(r, vo, so, AUX); }
template <int AUX> __device__ __forceinline__ void st32(u32 v, rsrc_t r, u32 vo, u32 so) { __builtin_amdgcn_raw_buffer_store_b32(v, r, vo, so, AUX); }
template <int AUX> __device__ __forceinline__ void st64(v2u v, rsrc_t r, u32 vo, u32 so) { __builtin_amdgcn_raw_buffer_store_b64(v, r, vo, so, AUX); }
template <int AUX> __device__ __forceinline__ void st128(v4u v, rsrc_t r, u32 vo, u32 so) { __builtin_amdgcn_raw_buffer_store_b128(v, r, vo + so, 0, AUX); }
#define FENCE() asm volatile("" ::: "memory")
#define DRAIN() asm volatile("s_waitcnt vmcnt(0)" ::: "memory")
__device__ __forceinline__ void bar() {
    __builtin_amdgcn_sched_barrier(0);
    asm volatile("s_waitcnt lgkmcnt(0)" ::: "memory");
    __builtin_amdgcn_s_barrier();
    FENCE();
    __builtin_amdgcn_sched_barrier(0);
}
template <int CTRL> __device__ __forceinline__ u32 dpp(u32 x) { return (u32)__builtin_amdgcn_update_dpp(0, (int)x, CTRL, 0xF, 0xF, true); }
__device__ __forceinline__ float sum32(float x) {
    x += asf(dpp<0xB1>(asu(x)));
    x += asf(dpp<0x4E>(asu(x)));
    x += asf(dpp<0x141>(asu(x)));
    x += asf(dpp<0x140>(asu(x)));
    auto r = __builtin_amdgcn_permlane16_swap(asu(x), asu(x), false, false);
    return asf(r[0]) + asf(r[1]);
}
__device__ __forceinline__ u32 umax(u32 a, u32 b) { return a > b ? a : b; }
__device__ __forceinline__ u32 umin(u32 a, u32 b) { return a < b ? a : b; }
__device__ __forceinline__ u32 max64(u32 x) {
    x = umax(x, dpp<0xB1>(x));
    x = umax(x, dpp<0x4E>(x));
    x = umax(x, dpp<0x141>(x));
    x = umax(x, dpp<0x140>(x));
    auto r = __builtin_amdgcn_permlane16_swap(x, x, false, false);
    x = umax(r[0], r[1]);
    auto s = __builtin_amdgcn_permlane32_swap(x, x, false, false);
    return umax(s[0], s[1]);
}
__device__ __forceinline__ f4 mfma(v4u a, v4u b, f4 c) {
    return __builtin_amdgcn_mfma_f32_16x16x32_bf16(__builtin_bit_cast(bf8, a), __builtin_bit_cast(bf8, b), c, 0, 0, 0);
}

#define STAMP(i) do { if ((i) == PROBE_A || (i) == PROBE_B) { __builtin_amdgcn_sched_barrier(0); ts[i] = (u32)__builtin_amdgcn_s_memrealtime(); __builtin_amdgcn_sched_barrier(0); } } while (0)

__shared__ __attribute__((aligned(16))) char lds_raw[L_END];

__device__ __forceinline__ LAS char* L(int off) { return (LAS char*)lds_raw + off; }
__device__ __forceinline__ void cset(int w) { *(volatile LAS u32*)L(L_C + 4 * w) = 1u; }
__device__ __forceinline__ void cwait(int w) {
    for (u32 n = 0; n < (1u << 22); ++n) {
        if (*(volatile LAS u32*)L(L_C + 4 * w)) break;
        __builtin_amdgcn_s_sleep(1);
    }
    FENCE();
}
__device__ __forceinline__ void wait_flags(rsrc_t rw, int base, u32 tlo, u32 thi, int lane) {
    for (u32 n = 0; n < SPIN_CAP; ++n) {
        FENCE();
        bool ok = true;
#pragma unroll
        for (int i = 0; i < 4; ++i) {
            const v2u f = ld64<SC1>(rw, (lane + 64 * i) * 128, base);
            ok &= f.x == tlo && f.y == thi;
        }
        if (__all(ok)) break;
        __builtin_amdgcn_s_sleep(4);
    }
    FENCE();
}
__device__ __forceinline__ void wait_word(rsrc_t rw, u32 off) {
    for (u32 n = 0; n < SPIN_CAP; ++n) {
        FENCE();
        if (ld32<SC1>(rw, 0, off) != SENT) break;
        __builtin_amdgcn_s_sleep(2);
    }
    FENCE();
}
template <int N> __device__ __forceinline__ void poll16(rsrc_t rw, const u32 (&vo)[N], u32 so, v4u (&v)[N]) {
    bool ok[N];
#pragma unroll
    for (int i = 0; i < N; ++i) ok[i] = false;
    for (u32 n = 0; n < SPIN_CAP; ++n) {
        FENCE();
#pragma unroll
        for (int i = 0; i < N; ++i) if (!ok[i]) v[i] = ld128<SC1>(rw, vo[i], so);
        bool all = true;
#pragma unroll
        for (int i = 0; i < N; ++i) { ok[i] = valid4(v[i]); all &= ok[i]; }
        if (__all(all)) break;
    }
    FENCE();
}

#define DSR64(dst, addr, off) asm volatile("ds_read_b64 %0, %1 offset:%2" : "=v"(dst) : "v"(addr), "i"(off))
#define DSR128(dst, addr, off) asm volatile("ds_read_b128 %0, %1 offset:%2" : "=v"(dst) : "v"(addr), "i"(off))
#define SB() __builtin_amdgcn_sched_barrier(0)
#define MFMA_IP(acc, a, b) asm volatile("v_mfma_f32_16x16x32_bf16 %0, %1, %2, %0" : "+v"(acc) : "v"(a), "v"(b))

struct AF { v2u lo, hi; };
__device__ __forceinline__ v4u cat(const AF& a) { return v4u{a.lo.x, a.lo.y, a.hi.x, a.hi.y}; }

template <int I0> __device__ __forceinline__ void conv_phase(u32 va, u32 vb, f4 (&acc)[8], AF (&alo)[4], v4u (&blo)[4], AF (&alo2)[4], v4u (&blo2)[4]) {
    AF ahi[4];
    v4u bhi[4];
    constexpr int OA = 64 * I0, OB = 448 - 64 * I0;
    asm volatile("s_waitcnt lgkmcnt(0)" : "+v"(alo[0].lo), "+v"(alo[0].hi), "+v"(alo[1].lo), "+v"(alo[1].hi), "+v"(alo[2].lo), "+v"(alo[2].hi), "+v"(alo[3].lo),
                 "+v"(alo[3].hi), "+v"(blo[0]), "+v"(blo[1]), "+v"(blo[2]), "+v"(blo[3]));
    SB();
#pragma unroll
    for (int n = 0; n < 16; ++n) {
        const int e = n >> 2, b = n & 3;
        MFMA_IP(acc[e + b], cat(alo[e]), blo[b]);
        if (n < 4) DSR128(bhi[n], vb, OB + 512 * (4 + n));
        else if (n < 12) {
            if (n & 1) DSR64(ahi[(n - 4) >> 1].hi, va, OA + 512 * (4 + ((n - 4) >> 1)) + 8);
            else DSR64(ahi[(n - 4) >> 1].lo, va, OA + 512 * (4 + ((n - 4) >> 1)));
        }
        SB();
    }
    asm volatile("s_waitcnt lgkmcnt(0)" : "+v"(ahi[0].lo), "+v"(ahi[0].hi), "+v"(ahi[1].lo), "+v"(ahi[1].hi), "+v"(ahi[2].lo), "+v"(ahi[2].hi), "+v"(ahi[3].lo),
                 "+v"(ahi[3].hi), "+v"(bhi[0]), "+v"(bhi[1]), "+v"(bhi[2]), "+v"(bhi[3]));
    SB();
    int n = 0;
#pragma unroll
    for (int half = 0; half < 2; ++half)
#pragma unroll
        for (int e = 0; e < 4; ++e)
#pragma unroll
            for (int b = 0; b < 4 - e; ++b) {
                if (half == 0) MFMA_IP(acc[4 + e + b], cat(alo[e]), bhi[b]);
                else MFMA_IP(acc[4 + e + b], cat(ahi[e]), blo[b]);
                if (I0 < 7) {
                    if (n < 4) DSR128(blo2[n], vb, OB - 64 + 512 * n);
                    else if (n < 12) {
                        if (n & 1) DSR64(alo2[(n - 4) >> 1].hi, va, OA + 64 + 512 * ((n - 4) >> 1) + 8);
                        else DSR64(alo2[(n - 4) >> 1].lo, va, OA + 64 + 512 * ((n - 4) >> 1));
                    }
                }
                ++n;
                SB();
            }
}

__global__ void __launch_bounds__(NT, 3) fused_kernel(Params p) {
    const int tid = threadIdx.x, lane = tid & 63, wave = __builtin_amdgcn_readfirstlane(tid >> 6);
    const int wg = blockIdx.x, xq = wg & 7, xi = wg >> 3, b = 32 * xq + xi;
    const bool isConv = xi < 18, isMoe = xi >= 18 && xi < 27;
    const int ridx = 5 * xq + xi - 27;
    const bool isRed = xi >= 27 && ridx < 36, isSel = xi >= 27 && ridx == 39;
    const int cj = 18 * xq + xi, mu = 9 * xq + xi - 18;
    constexpr int SELWG = 255;
    u32 ts[12];
#pragma unroll
    for (int i = 0; i < 12; ++i) ts[i] = 0;
    STAMP(0);
    const u64 pad0 = __builtin_amdgcn_s_memrealtime();
    asm volatile("" ::"s"(p.q), "s"(p.k), "s"(p.v), "s"(p.sk), "s"(p.h), "s"(p.dv), "s"(p.wr), "s"(p.wg), "s"(p.wu), "s"(p.wd), "s"(p.out), "s"(p.ws));
    const rsrc_t rw = mkrs(p.ws, WS_BYTES);
    const rsrc_t rq = mkrs(p.q, S * D * 2);

    if (wave == 9) {
        const v4u s4 = {SENT, SENT, SENT, SENT};
        const v2u e = ld64<SC1>(rw, 0, W_E);
#pragma unroll
        for (int i = 0; i < 9; ++i) st128<SC1>(s4, rw, lane * 16 + 1024 * i, W_U + b * 9216);
        if (lane < 4) st128<SC1>(s4, rw, lane * 16, W_KEY + b * 64);
        if (isSel) {
#pragma unroll
            for (int i = 0; i < 9; ++i) st128<SC1>(s4, rw, lane * 16 + 1024 * i, W_XSEL);
            if (lane < 4) st128<SC1>(s4, rw, lane * 16, W_SEL);
        }
        if (lane < 16) *(LAS u32*)L(L_C + 4 * lane) = 0u;
        bar();
        const u64 tag = (((u64)e.y << 32) | e.x) + 1ull;
        const u32 tlo = __builtin_amdgcn_readfirstlane((u32)tag), thi = __builtin_amdgcn_readfirstlane((u32)(tag >> 32));
        DRAIN();
        STAMP(1);
        if (lane == 0) st64<SC1>(v2u{tlo, thi}, rw, 0, W_F1 + wg * 128);
        cset(C_CLR);
        if (isConv) {
            wait_flags(rw, W_F1, tlo, thi, lane);
            cset(C_F1);
            STAMP(2);
#pragma unroll
            for (int i = 0; i < 16; ++i) st128<SC1>(s4, rw, lane * 16 + 1024 * i, W_Y + cj * 16384);
            bar();
            DRAIN();
            if (lane == 0) st64<SC1>(v2u{tlo, thi}, rw, 0, W_F2 + wg * 128);
            STAMP(3);
            wait_flags(rw, W_F2, tlo, thi, lane);
            cset(C_F2);
            STAMP(4);
            bar();
        } else if (isMoe) {
#pragma unroll
            for (int i = 0; i < 18; ++i) st128<SC1>(s4, rw, lane * 16 + 1024 * i, W_PART + mu * 18432);
            DRAIN();
            if (lane == 0) st64<SC1>(v2u{tlo, thi}, rw, 0, W_F2 + wg * 128);
            STAMP(2);
            for (u32 n = 0; n < SPIN_CAP; ++n) {
                FENCE();
                const v2u f = ld64<SC1>(rw, 0, W_F1 + SELWG * 128);
                if (f.x == tlo && f.y == thi) break;
                __builtin_amdgcn_s_sleep(4);
            }
            wait_word(rw, W_XSEL + 9212);
            STAMP(3);
            u32 vo[9];
            v4u xv[9];
#pragma unroll
            for (int i = 0; i < 9; ++i) vo[i] = (lane + 64 * i) * 16;
            poll16<9>(rw, vo, W_XSEL, xv);
#pragma unroll
            for (int i = 0; i < 9; ++i) {
                const int c = lane + 64 * i;
                *(LAS v4u*)L(L_XS + (c / 72) * XSS + (c % 72) * 16) = xv[i];
            }
            STAMP(4);
            bar();
            bar();
            bar();
            wait_flags(rw, W_F2, tlo, thi, lane);
            cset(C_F2);
        } else {
            if (lane == 0) st64<SC1>(v2u{tlo, thi}, rw, 0, W_F2 + wg * 128);
            if (isSel) {
                wait_flags(rw, W_F1, tlo, thi, lane);
                if (lane == 0) st64<SC1>(v2u{tlo, thi}, rw, 0, W_E);
                STAMP(2);
                bar();
                bar();
                bar();
                bar();
            } else {
                bar();
            }
            wait_flags(rw, W_F2, tlo, thi, lane);
            cset(C_F2);
            STAMP(4);
        }
        if (!isSel) {
            u32 vo[1] = {(u32)(lane & 3) * 16};
            v4u sv[1];
            poll16<1>(rw, vo, W_SEL, sv);
            if (lane < 4) *(LAS v4u*)L(L_SEL + lane * 16) = sv[0];
            asm volatile("s_waitcnt lgkmcnt(0)" ::: "memory");
        }
        cset(C_SEL);
        STAMP(5);
    } else {
        const bool isK = wave < 5;
        const int cp = tid;
        const int lw = wave - 5;
        v4u hd[4];
        v2u hn[4];
        if (isK) {
            const rsrc_t rk = mkrs(p.k, S * D * 2), rv = mkrs(p.v, S * D * 2);
            const u32 vo = tid < 288 ? tid * 4 : 0x40000000u;
            u32 kk[11], vv[8], qq[8];
            v4u kvc[2];
#pragma unroll
            for (int i = 0; i < 11; ++i) {
                const int row = 8 * b - 3 + i;
                kk[i] = ld32<AUX_IN>(rk, vo, row >= 0 ? row * 1152 : 0x40000000);
            }
#pragma unroll
            for (int i = 0; i < 8; ++i) vv[i] = ld32<AUX_IN>(rv, vo, (8 * b + i) * 1152);
            const v4u sk = ld128<0>(mkrs(p.sk, D * 8), tid * 16, 0);
#pragma unroll
            for (int i = 0; i < 8; ++i) qq[i] = ld32<AUX_IN>(rq, vo, (8 * b + i) * 1152);
            const u32 dvp = ld32<0>(mkrs(p.dv, D * 2), vo, 0);
            bar();
            const f2 w0 = {lo16(sk.x), lo16(sk.z)}, w1 = {hi16(sk.x), hi16(sk.z)}, w2 = {lo16(sk.y), lo16(sk.w)}, w3 = {hi16(sk.y), hi16(sk.w)};
            f2 kf[11], r[8];
#pragma unroll
            for (int i = 0; i < 11; ++i) kf[i] = unpk(kk[i]);
#pragma unroll
            for (int t = 0; t < 8; ++t) r[t] = (kf[t] * w3 + kf[t + 1] * w2 + kf[t + 2] * w1 + kf[t + 3] * w0) * unpk(vv[t]);
            kvc[0] = v4u{nos(pk(r[7].x, r[6].x)), nos(pk(r[5].x, r[4].x)), nos(pk(r[3].x, r[2].x)), nos(pk(r[1].x, r[0].x))};
            kvc[1] = v4u{nos(pk(r[7].y, r[6].y)), nos(pk(r[5].y, r[4].y)), nos(pk(r[3].y, r[2].y)), nos(pk(r[1].y, r[0].y))};
            STAMP(1);
            cwait(C_CLR);
            if (tid < 288) {
                st128<SC1>(kvc[0], rw, tid * 16, W_U + b * 9216);
                st128<SC1>(kvc[1], rw, tid * 16, W_U + b * 9216 + 4608);
            }
            cset(C_UST);
            STAMP(2);
            *(LAS v4u*)L(L_ST + tid * 80) = v4u{qq[0], qq[1], qq[2], qq[3]};
            *(LAS v4u*)L(L_ST + tid * 80 + 16) = v4u{qq[4], qq[5], qq[6], qq[7]};
            *(LAS v4u*)L(L_ST + tid * 80 + 32) = kvc[0];
            *(LAS v4u*)L(L_ST + tid * 80 + 48) = kvc[1];
            *(LAS u32*)L(L_ST + tid * 80 + 64) = dvp;
        } else {
            const int hf = lane >> 5, l32 = lane & 31, tl = 2 * lw + hf, tok = 8 * b + tl;
            const rsrc_t rr = mkrs(p.wr, D * 2);
            u32 xq9[9], wr9[9];
#pragma unroll
            for (int i = 0; i < 9; ++i) xq9[i] = ld32<AUX_IN>(rq, tok * 1152 + l32 * 4, 128 * i);
#pragma unroll
            for (int i = 0; i < 9; ++i) wr9[i] = ld32<0>(rr, l32 * 4, 128 * i);
            {
                const int c = 4 * cj + lw, d = c < 288 ? 2 * c : 2 * (c - 288) + 1;
                const rsrc_t rh = mkrs(p.h, isConv ? D * S * 2 : 0);
#pragma unroll
                for (int i = 0; i < 4; ++i) hd[i] = ld128<AUX_IN>(rh, lane * 16 + 1024 * i, d * 4096);
#pragma unroll
                for (int i = 0; i < 4; ++i) hn[i] = ld64<AUX_IN>(rh, lane * 16 + 1024 * i + 16, d * 4096);
            }
            bar();
            f2 x[9];
            f2 s2 = {0.f, 0.f};
#pragma unroll
            for (int i = 0; i < 9; ++i) { x[i] = unpk(xq9[i]); s2 += x[i]; }
            const float mean = sum32(s2.x + s2.y) * (1.0f / D);
            f2 v2 = {0.f, 0.f};
#pragma unroll
            for (int i = 0; i < 9; ++i) { const f2 dl = x[i] - mean; v2 += dl * dl; }
            const float var = rbf(sum32(v2.x + v2.y) * (1.0f / D));
            const float mb = rbf(mean), sd = rbf(__builtin_amdgcn_sqrtf(rbf(var + 1e-5f)));
            const float inv = __builtin_amdgcn_rcpf(sd);
            f2 a2 = {0.f, 0.f};
#pragma unroll
            for (int i = 0; i < 9; ++i) a2 += rbf2(rbf2(x[i] - mb) * inv) * unpk(wr9[i]);
            const float rs = sum32(a2.x + a2.y);
            const u32 rb = pk(rs, rs) & 0xFFFFu;
            u32 ord = (rb & 0x8000u) ? (~rb & 0xFFFFu) : (rb | 0x8000u);
            if ((rb & 0x7FFFu) > 0x7F80u) ord = 0u;
            const v2u rec = {(ord << 16) | (u32)(2047 - tok), nos(pk(mb, sd))};
            STAMP(1);
            cwait(C_CLR);
            if (l32 == 0) {
                st64<SC1>(rec, rw, tl * 8, W_KEY + b * 64);
                *(LAS f2*)L(L_STAT + tl * 8) = f2{mb, inv};
            }
            STAMP(2);
        }

        if (isConv) {
            if (isK) {
                bar();
            } else {
                LAS char* hb = L(L_H + lw * HCH);
#pragma unroll
                for (int i = 0; i < 4; ++i) {
                    const v4u dd = hd[i];
                    const v2u nn = hn[i];
                    const u32 a01 = __builtin_amdgcn_alignbit(dd.y, dd.x, 16), a12 = __builtin_amdgcn_alignbit(dd.z, dd.y, 16),
                              a23 = __builtin_amdgcn_alignbit(dd.w, dd.z, 16), a3n = __builtin_amdgcn_alignbit(nn.x, dd.w, 16),
                              ann = __builtin_amdgcn_alignbit(nn.y, nn.x, 16);
                    LAS char* o = hb + (lane + 64 * i + 2) * 16;
                    *(LAS v4u*)(o) = dd;
                    *(LAS v4u*)(o + HC) = v4u{a01, a12, a23, a3n};
                    *(LAS v4u*)(o + 2 * HC) = v4u{dd.y, dd.z, dd.w, nn.x};
                    *(LAS v4u*)(o + 3 * HC) = v4u{a12, a23, a3n, ann};
                }
                {
                    const u32 h0 = __builtin_amdgcn_readfirstlane(hd[0].x), h1 = __builtin_amdgcn_readfirstlane(hd[0].y);
                    if (lane < 8) {
                        const int sg = lane >> 1;
                        v4u z = {0u, 0u, 0u, 0u};
                        if (lane & 1) {
                            if (sg == 1) z.w = h0 << 16;
                            else if (sg == 2) z.w = h0;
                            else if (sg == 3) { z.z = h0 << 16; z.w = (h0 >> 16) | (h1 << 16); }
                        }
                        *(LAS v4u*)(hb + sg * HC + (lane & 1) * 16) = z;
                    }
                    if (lane < 32) *(LAS v4u*)L(L_U + lw * UCH + lane * 16) = v4u{0u, 0u, 0u, 0u};
                }
                f4 acc[8];
#pragma unroll
                for (int i = 0; i < 8; ++i) {
                    acc[i] = f4{0.f, 0.f, 0.f, 0.f};
                    asm volatile("" : "+v"(acc[i]));
                }
                STAMP(3);
                cwait(C_F1);
                cwait(C_UST);
                if (U_DELAY) __builtin_amdgcn_s_sleep(U_DELAY);
                STAMP(4);
                {
                    const int l = tid - 320;
                    u32 vo[4];
                    v4u uv[4];
#pragma unroll
                    for (int i = 0; i < 4; ++i) vo[i] = (64 * i + (l >> 2)) * 9216 + (l & 3) * 16;
                    poll16<4>(rw, vo, W_U + cj * 64, uv);
#pragma unroll
                    for (int i = 0; i < 4; ++i) *(LAS v4u*)L(L_U + (l & 3) * UCH + (32 + 64 * i + (l >> 2)) * 16) = uv[i];
                }
                STAMP(5);
                bar();
                const int m1 = (lane & 15) + 1, kq = lane >> 4;
                const u32 va = (u32)(uintptr_t)(hb + (m1 & 3) * HC + 16 * kq + 8 * (m1 >> 2));
                const u32 vb = (u32)(uintptr_t)L(L_U + lw * UCH + (33 + 2 * (lane & 15) - kq) * 16 - 448);
                AF a0[4], a1[4];
                v4u b0[4], b1[4];
#pragma unroll
                for (int n = 0; n < 4; ++n) {
                    DSR128(b0[n], vb, 448 + 512 * n);
                    DSR64(a0[n].lo, va, 512 * n);
                    DSR64(a0[n].hi, va, 512 * n + 8);
                }
                conv_phase<0>(va, vb, acc, a0, b0, a1, b1);
                conv_phase<1>(va, vb, acc, a1, b1, a0, b0);
                conv_phase<2>(va, vb, acc, a0, b0, a1, b1);
                conv_phase<3>(va, vb, acc, a1, b1, a0, b0);
                conv_phase<4>(va, vb, acc, a0, b0, a1, b1);
                conv_phase<5>(va, vb, acc, a1, b1, a0, b0);
                conv_phase<6>(va, vb, acc, a0, b0, a1, b1);
                conv_phase<7>(va, vb, acc, a1, b1, a0, b0);
                asm volatile("s_nop 7\n\ts_nop 1" : "+v"(acc[0]), "+v"(acc[1]), "+v"(acc[2]), "+v"(acc[3]), "+v"(acc[4]), "+v"(acc[5]), "+v"(acc[6]), "+v"(acc[7]));
                STAMP(6);
#pragma unroll
                for (int q = 0; q < 8; q += 2) {
                    const u32 y0 = nos(pk(acc[q][0], acc[q][1])), y1 = nos(pk(acc[q][2], acc[q][3]));
                    const u32 z0 = nos(pk(acc[q + 1][0], acc[q + 1][1])), z1 = nos(pk(acc[q + 1][2], acc[q + 1][3]));
                    const auto s0 = __builtin_amdgcn_permlane16_swap(y0, z0, false, false);
                    const auto s1 = __builtin_amdgcn_permlane16_swap(y1, z1, false, false);
                    const int blk = 32 * (q + (kq & 1)) + 2 * (lane & 15) + (kq >> 1);
                    *(LAS v4u*)L(L_Y + blk * 64 + ((lw ^ (blk >> 1)) & 3) * 16) = v4u{s0[0], s1[0], s0[1], s1[1]};
                }
                bar();
                {
                    const int l = tid - 320;
#pragma unroll
                    for (int i = 0; i < 4; ++i) {
                        const int c = l + 256 * i, blk = c >> 2;
                        const v4u yv = *(LAS v4u*)L(L_Y + blk * 64 + (((c & 3) ^ (blk >> 1)) & 3) * 16);
                        st128<SC1>(yv, rw, c * 16, W_Y + cj * 16384);
                    }
                }
                STAMP(7);
            }
        } else if (isMoe) {
            const int c = lane & 15, kq = lane >> 4;
            const GAS char* gp = (const GAS char*)(c < 8 ? p.wg : p.wu) + (size_t)(64 * wave + 8 * kq) * (F * 2) + mu * 128 + (c & 7) * 16;
            v4u gr[2][8];
            v2u dr[2][8];
#pragma unroll
            for (int ks = 0; ks < 2; ++ks)
#pragma unroll
                for (int e = 0; e < 8; ++e) {
                    const GAS v4u* a = (const GAS v4u*)(gp + (size_t)(32 * ks + e) * (F * 2));
                    gr[ks][e] = AUX_W ? __builtin_nontemporal_load(a) : *a;
                }
            const rsrc_t rd = mkrs(p.wd, F * D * 2);
#pragma unroll
            for (int ks = 0; ks < 2; ++ks)
#pragma unroll
                for (int e = 0; e < 8; ++e) dr[ks][e] = ld64<AUX_W>(rd, kq * (8 * 1152) + c * 8, (64 * mu + 32 * ks + e) * 1152 + wave * 128);
            STAMP(3);
            v4u gf[2][8];
#pragma unroll
            for (int ks = 0; ks < 2; ++ks)
#pragma unroll
                for (int nn = 0; nn < 8; ++nn)
#pragma unroll
                    for (int pp = 0; pp < 4; ++pp)
                        gf[ks][nn][pp] = __builtin_amdgcn_perm(gr[ks][2 * pp + 1][nn >> 1], gr[ks][2 * pp][nn >> 1], (nn & 1) ? 0x07060302u : 0x05040100u);
#pragma unroll
            for (int ks = 0; ks < 2; ++ks)
#pragma unroll
                for (int nn = 0; nn < 8; ++nn) asm volatile("" : "+v"(gf[ks][nn]));
            STAMP(4);
            bar();
            STAMP(5);
            {
                v4u xa[2];
#pragma unroll
                for (int ks = 0; ks < 2; ++ks) {
                    xa[ks] = v4u{0u, 0u, 0u, 0u};
                    if (c < 8) xa[ks] = *(LAS v4u*)L(L_XS + c * XSS + (64 * wave + 32 * ks + 8 * kq) * 2);
                }
                f4 g[8];
#pragma unroll
                for (int nn = 0; nn < 8; ++nn) g[nn] = mfma(xa[1], gf[1][nn], mfma(xa[0], gf[0][nn], f4{0.f, 0.f, 0.f, 0.f}));
                if (kq < 2) {
#pragma unroll
                    for (int r = 0; r < 4; ++r) {
                        LAS char* o = L(L_R + ((wave * 8 + 4 * kq + r) * 128 + 8 * c) * 4);
                        *(LAS f4*)o = f4{g[0][r], g[1][r], g[2][r], g[3][r]};
                        *(LAS f4*)(o + 16) = f4{g[4][r], g[5][r], g[6][r], g[7][r]};
                    }
                }
            }
            bar();
            if (wave < 8) {
                float g = 0.f, u = 0.f;
#pragma unroll
                for (int w = 0; w < 9; ++w) {
                    g += *(LAS float*)L(L_R + ((w * 8 + wave) * 128 + lane) * 4);
                    u += *(LAS float*)L(L_R + ((w * 8 + wave) * 128 + 64 + lane) * 4);
                }
                g = rbf(g);
                u = rbf(u);
                const float sg = rbf(g * __builtin_amdgcn_rcpf(1.0f + __expf(-g)));
                *(LAS u16*)L(L_HA + wave * HAS + lane * 2) = (u16)pk(sg * u, 0.f);
            }
            v4u df[2][4];
#pragma unroll
            for (int ks = 0; ks < 2; ++ks)
#pragma unroll
                for (int nn = 0; nn < 4; ++nn)
#pragma unroll
                    for (int pp = 0; pp < 4; ++pp)
                        df[ks][nn][pp] = __builtin_amdgcn_perm(dr[ks][2 * pp + 1][nn >> 1], dr[ks][2 * pp][nn >> 1], (nn & 1) ? 0x07060302u : 0x05040100u);
            bar();
            STAMP(6);
            {
                v4u ha[2];
#pragma unroll
                for (int ks = 0; ks < 2; ++ks) {
                    ha[ks] = v4u{0u, 0u, 0u, 0u};
                    if (c < 8) ha[ks] = *(LAS v4u*)L(L_HA + c * HAS + (32 * ks + 8 * kq) * 2);
                }
                f4 y[4];
#pragma unroll
                for (int nn = 0; nn < 4; ++nn) y[nn] = mfma(ha[1], df[1][nn], mfma(ha[0], df[0][nn], f4{0.f, 0.f, 0.f, 0.f}));
                if (kq < 2) {
#pragma unroll
                    for (int r = 0; r < 4; ++r)
                        st128<SC1>(v4u{nos(asu(y[0][r])), nos(asu(y[1][r])), nos(asu(y[2][r])), nos(asu(y[3][r]))}, rw, (4 * kq + r) * 2304 + c * 16,
                                   W_PART + mu * 18432 + wave * 256);
                }
            }
            STAMP(7);
        } else if (isSel) {
            if (tid == 0) *(LAS u32*)L(L_C + 4 * C_CNT) = 0u;
            bar();
            const int sw = wave < 4 ? wave : wave - 1;
            v4u kv2[2];
            if (wave != 4) {
                u32 vo[2] = {(u32)(sw * 64 + lane) * 16, (u32)(sw * 64 + lane) * 16 + 8192};
                poll16<2>(rw, vo, W_KEY, kv2);
                const u32 mx = max64(umax(umax(kv2[0].x, kv2[0].z), umax(kv2[1].x, kv2[1].z)));
                if (lane == 0) *(LAS u32*)L(L_WM + 4 * sw) = mx;
            }
            STAMP(3);
            bar();
            if (wave != 4) {
                const v4u m0 = *(LAS v4u*)L(L_WM), m1 = *(LAS v4u*)L(L_WM + 16);
                const u32 thr = umin(umin(umin(m0.x, m0.y), umin(m0.z, m0.w)), umin(umin(m1.x, m1.y), umin(m1.z, m1.w)));
#pragma unroll
                for (int i = 0; i < 4; ++i) {
                    const u32 key = (i & 1) ? kv2[i >> 1].z : kv2[i >> 1].x, ms = (i & 1) ? kv2[i >> 1].w : kv2[i >> 1].y;
                    if (key >= thr) {
                        const u32 pos = __hip_atomic_fetch_add((LAS u32*)L(L_C + 4 * C_CNT), 1u, __ATOMIC_RELAXED, __HIP_MEMORY_SCOPE_WORKGROUP);
                        *(LAS v2u*)L(L_LIST + pos * 8) = v2u{key, ms};
                    }
                }
            }
            bar();
            {
                const int n = *(LAS u32*)L(L_C + 4 * C_CNT);
                for (int i = tid; i < n; i += 576) {
                    const v2u me = *(LAS v2u*)L(L_LIST + i * 8);
                    int rank = 0;
                    for (int j = 0; j < n; ++j) rank += *(LAS u32*)L(L_LIST + j * 8) > me.x;
                    if (rank < 8) *(LAS v2u*)L(L_SEL + rank * 8) = me;
                }
            }
            bar();
            STAMP(4);
            {
                const int s = tid / 72, ch = tid % 72;
                const v2u me = *(LAS v2u*)L(L_SEL + s * 8);
                const int tok = (2047 - me.x) & 2047;
                const v4u xr = ld128<0>(rq, tok * 1152 + ch * 16, 0);
                const float mb = lo16(me.y), inv = __builtin_amdgcn_rcpf(hi16(me.y));
                v4u o;
#pragma unroll
                for (int i = 0; i < 4; ++i) {
                    const f2 t = rbf2(unpk(xr[i]) - mb) * inv;
                    o[i] = nos(pk(t.x, t.y));
                }
                st128<SC1>(o, rw, tid * 16, W_XSEL);
                if (tid < 4) st128<SC1>(*(LAS v4u*)L(L_SEL + tid * 16), rw, tid * 16, W_SEL);
            }
            STAMP(5);
        } else {
            bar();
            if (isRed && !isK) {
                const int rho = ridx * 4 + lw, s = rho / 18, cb = rho % 18, d = 32 * cb + (lane & 31);
                cwait(C_SEL);
                STAMP(3);
                const v2u me = *(LAS v2u*)L(L_SEL + s * 8);
                const int tok = (2047 - me.x) & 2047;
                const u32 ord = me.x >> 16, rb = (ord & 0x8000u) ? (ord & 0x7FFFu) : (~ord & 0xFFFFu);
                const float mb = lo16(me.y), inv = __builtin_amdgcn_rcpf(hi16(me.y));
                const float x = lo16(p.q[tok * D + d]);
                float ks = 0.f;
#pragma unroll
                for (int j = 3; j >= 0; --j) ks += (tok - j >= 0 ? lo16(p.k[(tok - j) * D + d]) : 0.f) * lo16(p.sk[d * 4 + j]);
                const float kv = rbf(ks * lo16(p.v[tok * D + d]));
                const float xn = rbf(rbf(x - mb) * inv);
                const float kd = kv * lo16(p.dv[d]);
                const float rsg = rbf(__builtin_amdgcn_rcpf(1.0f + __expf(-lo16(rb))));
                cwait(C_F2);
                wait_word(rw, W_PART + 71 * 18432 + s * 2304 + cb * 128);
                STAMP(4);
                u32 vo[9];
                v4u pv[9];
#pragma unroll
                for (int i = 0; i < 9; ++i) vo[i] = (8 * i + (lane >> 3)) * 18432 + (lane & 7) * 16;
                poll16<9>(rw, vo, W_PART + s * 2304 + cb * 128, pv);
                f4 a = {0.f, 0.f, 0.f, 0.f};
#pragma unroll
                for (int i = 0; i < 9; ++i) a += __builtin_bit_cast(f4, pv[i]);
#pragma unroll
                for (int i = 0; i < 4; ++i) {
                    float t = a[i];
                    t += asf(dpp<0x128>(asu(t)));
                    const auto r1 = __builtin_amdgcn_permlane16_swap(asu(t), asu(t), false, false);
                    t = asf(r1[0]) + asf(r1[1]);
                    const auto r2 = __builtin_amdgcn_permlane32_swap(asu(t), asu(t), false, false);
                    a[i] = asf(r2[0]) + asf(r2[1]);
                }
                if (lane < 8) *(LAS f4*)L(L_RED + lw * 128 + lane * 16) = a;
                asm volatile("s_waitcnt lgkmcnt(0)" ::: "memory");
                const float moe = rbf(rbf(*(LAS float*)L(L_RED + lw * 128 + (lane & 31) * 4)) * rsg);
                STAMP(5);
                const int c = (d & 1) * 288 + (d >> 1);
                const u32 yo = (c >> 2) * 16384 + (c & 3) * 16 + (tok >> 3) * 64 + ((tok & 7) >> 1) * 4;
                u32 yw = SENT;
                for (u32 n = 0; n < SPIN_CAP; ++n) {
                    FENCE();
                    yw = ld32<SC1>(rw, yo, W_Y);
                    if (__all(yw != SENT)) break;
                    __builtin_amdgcn_s_sleep(2);
                }
                const float y = (tok & 1) ? hi16(yw) : lo16(yw);
                const float o = x + xn * (y + kd) + rbf(xn + moe);
                if (lane < 32) p.out[tok * D + d] = (u16)pk(o, 0.f);
                STAMP(6);
            }
        }

        if (isK) {
            f2 xn[8], pb[8];
            const v4u qa = *(LAS v4u*)L(L_ST + tid * 80), qb = *(LAS v4u*)L(L_ST + tid * 80 + 16);
            const u32 qq[8] = {qa.x, qa.y, qa.z, qa.w, qb.x, qb.y, qb.z, qb.w};
            const v4u kvc[2] = {*(LAS v4u*)L(L_ST + tid * 80 + 32), *(LAS v4u*)L(L_ST + tid * 80 + 48)};
            const f2 dv = unpk(*(LAS u32*)L(L_ST + tid * 80 + 64));
#pragma unroll
            for (int t = 0; t < 8; ++t) {
                const f2 st = *(LAS f2*)L(L_STAT + t * 8);
                const f2 x = unpk(qq[t]);
                xn[t] = rbf2(rbf2(x - st.x) * st.y);
                const u32 c0 = kvc[0][(7 - t) >> 1], c1 = kvc[1][(7 - t) >> 1];
                const f2 kv = (t & 1) ? f2{lo16(c0), lo16(c1)} : f2{hi16(c0), hi16(c1)};
                pb[t] = x + xn[t] + xn[t] * (kv * dv);
            }
#pragma unroll
            for (int t = 0; t < 8; ++t) asm volatile("" : "+v"(xn[t]), "+v"(pb[t]));
            STAMP(8);
            if (isConv) {
                bar();
                if (Y_DELAY) __builtin_amdgcn_s_sleep(Y_DELAY);
            } else {
                cwait(C_F2);
                wait_word(rw, W_Y + (18 * xq + 17) * 16384 + b * 64);
            }
            STAMP(9);
            if (tid < 288) {
                u32 vo[2] = {(u32)((cp >> 2) * 16384 + (cp & 3) * 16), (u32)(((cp + 288) >> 2) * 16384 + (cp & 3) * 16)};
                v4u yv[2];
                poll16<2>(rw, vo, W_Y + b * 64, yv);
                STAMP(10);
                u32 o[8];
#pragma unroll
                for (int t = 0; t < 8; ++t) {
                    const u32 c0 = yv[0][t >> 1], c1 = yv[1][t >> 1];
                    const f2 y = (t & 1) ? f2{hi16(c0), hi16(c1)} : f2{lo16(c0), lo16(c1)};
                    const f2 r = pb[t] + xn[t] * y;
                    o[t] = pk(r.x, r.y);
                }
                cwait(C_SEL);
                u32 mask = 0;
                {
                    const v4u s0 = *(LAS v4u*)L(L_SEL), s1 = *(LAS v4u*)L(L_SEL + 16), s2 = *(LAS v4u*)L(L_SEL + 32), s3 = *(LAS v4u*)L(L_SEL + 48);
                    const u32 ky[8] = {s0.x, s0.z, s1.x, s1.z, s2.x, s2.z, s3.x, s3.z};
#pragma unroll
                    for (int i = 0; i < 8; ++i) {
                        const u32 rel = (2047u - (ky[i] & 0xFFFFu)) - 8u * b;
                        mask |= rel < 8u ? 1u << rel : 0u;
                    }
                    mask = __builtin_amdgcn_readfirstlane(mask);
                }
                const rsrc_t ro = mkrs(p.out, S * D * 2);
#pragma unroll
                for (int t = 0; t < 8; ++t)
                    if (!((mask >> t) & 1u)) st32<AUX_OUT>(o[t], ro, tid * 4, (8 * b + t) * 1152);
                STAMP(11);
            }
        }
    }
    {
        u32 extra = 0;
        if (PROBE_COND) extra = 10 * (ts[PROBE_B] - ts[PROBE_A]);
        if (extra > 30000) extra = 30000;
        while (__builtin_amdgcn_s_memrealtime() - pad0 < PADUS * 100 + extra) __builtin_amdgcn_s_sleep(1);
    }
}

extern "C" void kernel_launch(void* const* d_in, const int* in_sizes, int n_in, void* d_out, int out_size, void* d_ws, size_t ws_size,
                              hipStream_t stream) {
    Params p;
    p.q = (const u16*)d_in[0];
    p.k = (const u16*)d_in[1];
    p.v = (const u16*)d_in[2];
    p.sk = (const u16*)d_in[3];
    p.h = (const u16*)d_in[4];
    p.dv = (const u16*)d_in[5];
    p.wr = (const u16*)d_in[7];
    p.wg = (const u16*)d_in[8];
    p.wu = (const u16*)d_in[9];
    p.wd = (const u16*)d_in[10];
    p.out = (u16*)d_out;
    p.ws = (char*)d_ws + (WS_AT_END ? ((ws_size - WS_BYTES) & ~(size_t)4095) : 0);
    fused_kernel<<<256, NT, 0, stream>>>(p);
}
```

```cpp
#define PADUS 40
#define PROBE_A 0
#define PROBE_B 4
#define PROBE_COND (isConv&&isL)
#define Conv&&isK:2
#include <hip/hip_runtime.h>
#include <stdint.h>

typedef unsigned short u16;
typedef unsigned u32;
typedef unsigned long long u64;
typedef u32 v4u __attribute__((ext_vector_type(4)));
typedef u32 v2u __attribute__((ext_vector_type(2)));
typedef float f4 __attribute__((ext_vector_type(4)));
typedef float f2 __attribute__((ext_vector_type(2)));
typedef __bf16 bf8 __attribute__((ext_vector_type(8)));
typedef __bf16 bf2 __attribute__((ext_vector_type(2)));
#define GAS __attribute__((address_space(1)))
#define LAS __attribute__((address_space(3)))
typedef __amdgpu_buffer_rsrc_t rsrc_t;

#define AUX_IN 0
#define CLR_AUX 16
#define AUX_OUT 16
#define WS_AT_END 1
#define H_DELAY 0
#define Q_DELAY 4
#define U_DELAY 6
#define F_DELAY 20
#define K_DELAY 4
#define Y_DELAY 8
#define YDIRECT 0
#define X_DELAY 2
#define CODE_KB 27
#define HELP_C3 1
#define PF1 1
#define HELP_GU 1
#define HELP_DN 1

constexpr int S = 2048, D = 576, F = 4608;
constexpr int NT = 640;
constexpr u32 SENT = 0xFFFFFFFFu;
constexpr int SC1 = 16;
constexpr u32 SPIN_CAP = 1u << 16;
constexpr u32 OOB = 0x40000000u;

constexpr int XSB = 9344;
constexpr int W_E = 0, W_F3 = 1024, W_F1 = 4096, W_F2 = W_F1 + 2048, W_KEY = W_F2 + 2048, W_XS = W_KEY + 256 * 64, W_U = W_XS + 8 * XSB,
              W_Y = W_U + 256 * 9216, W_PART = W_Y + 144 * 16384, W_END = W_PART + 72 * 18432, W_DBG = W_END;
constexpr int WS_BYTES = W_END + (1 << 20);
static_assert(W_U % 128 == 0 && XSB % 128 == 0, "align");
constexpr int L_C = 0, L_STAT = L_C + 64, L_SEL = L_STAT + 64, L_WM = L_SEL + 64, L_RED = L_WM + 64, L_ST = 512, L_RA = L_ST + 320 * 32;
constexpr int HC = 4160, HCH = 4 * HC, L_H = L_RA, UCH = 290 * 16, L_U = L_H + 4 * HCH, L_Y = L_U + 4 * UCH;
constexpr int L_R = L_RA, L_HA = L_R + 9 * 8 * 128 * 4, HAS = 144, DNS = 1056, L_DN = L_HA + 1280, L_DT = L_DN + 64 * DNS, L_END = L_DT + 8192;
constexpr int L_LK = L_RA, L_LM = L_LK + 8192 + 128;
static_assert(L_Y + 16384 <= L_END && L_RA % 256 == 0 && L_DN % 256 == 0, "lds");
enum { C_CLR = 0, C_F1 = 1, C_F2 = 2, C_SEL = 3, C_UST = 4, C_CNT = 5, C_ST = 6, C_YST = 7, C_WI = 8, C_GO = 9, C_XS = 10, C_YL = 11, C_TLO = 12, C_THI = 13 };

struct Params {
    const u16 *q, *k, *v, *sk, *h, *dv, *wr, *wg, *wu, *wd;
    u16* out;
    char* ws;
};

__device__ __forceinline__ float asf(u32 x) { return __builtin_bit_cast(float, x); }
__device__ __forceinline__ u32 asu(float x) { return __builtin_bit_cast(u32, x); }
__device__ __forceinline__ float lo16(u32 x) { return asf(x << 16); }
__device__ __forceinline__ float hi16(u32 x) { return asf(x & 0xFFFF0000u); }
__device__ __forceinline__ f2 unpk(u32 x) { return f2{lo16(x), hi16(x)}; }
__device__ __forceinline__ u32 pk(float a, float b) { return __builtin_bit_cast(u32, __builtin_convertvector(f2{a, b}, bf2)); }
__device__ __forceinline__ u32 pk2(f2 a) { return pk(a.x, a.y); }
__device__ __forceinline__ float rbf(float a) { return lo16(pk(a, a)); }
__device__ __forceinline__ u32 nos(u32 x) { return x < 0xFFFFFFFEu ? x : 0xFFFFFFFEu; }
__device__ __forceinline__ u32 umax(u32 a, u32 b) { return a > b ? a : b; }
__device__ __forceinline__ u32 umin(u32 a, u32 b) { return a < b ? a : b; }
__device__ __forceinline__ bool valid4(v4u v) { return umax(umax(v.x, v.y), umax(v.z, v.w)) != SENT; }
__device__ __forceinline__ rsrc_t mkrs(const void* p, u32 bytes) { return __builtin_amdgcn_make_buffer_rsrc((void*)p, 0, bytes, 0x00020000); }
template <int AUX> __device__ __forceinline__ u32 ld32(rsrc_t r, u32 vo, u32 so) { return __builtin_amdgcn_raw_buffer_load_b32(r, vo, so, AUX); }
template <int AUX> __device__ __forceinline__ v2u ld64(rsrc_t r, u32 vo, u32 so) { return __builtin_amdgcn_raw_buffer_load_b64(r, vo, so, AUX); }
template <int AUX> __device__ __forceinline__ v4u ld128(rsrc_t r, u32 vo, u32 so) { return __builtin_amdgcn_raw_buffer_load_b128(r, vo, so, AUX); }
template <int AUX> __device__ __forceinline__ void st32(u32 v, rsrc_t r, u32 vo, u32 so) { __builtin_amdgcn_raw_buffer_store_b32(v, r, vo, so, AUX); }
template <int AUX> __device__ __forceinline__ void st64(v2u v, rsrc_t r, u32 vo, u32 so) { __builtin_amdgcn_raw_buffer_store_b64(v, r, vo, so, AUX); }
template <int AUX> __device__ __forceinline__ void st128(v4u v, rsrc_t r, u32 vo, u32 so) { __builtin_amdgcn_raw_buffer_store_b128(v, r, vo + so, 0, AUX); }
#define FENCE() asm volatile("" ::: "memory")
#define DRAIN() asm volatile("s_waitcnt vmcnt(0)" ::: "memory")
#define LGKM0() asm volatile("s_waitcnt lgkmcnt(0)" ::: "memory")
#define SB() __builtin_amdgcn_sched_barrier(0)
#define NOUNROLL _Pragma("unroll 1")
__device__ __forceinline__ void bar() {
    SB();
    LGKM0();
    __builtin_amdgcn_s_barrier();
    FENCE();
    SB();
}
template <int CTRL> __device__ __forceinline__ u32 dpp(u32 x) { return (u32)__builtin_amdgcn_update_dpp(0, (int)x, CTRL, 0xF, 0xF, true); }
__device__ __forceinline__ float sum32(float x) {
    x += asf(dpp<0xB1>(asu(x)));
    x += asf(dpp<0x4E>(asu(x)));
    x += asf(dpp<0x141>(asu(x)));
    x += asf(dpp<0x140>(asu(x)));
    auto r = __builtin_amdgcn_permlane16_swap(asu(x), asu(x), false, false);
    return asf(r[0]) + asf(r[1]);
}
__device__ __forceinline__ u32 max32(u32 x) {
    x = umax(x, dpp<0xB1>(x));
    x = umax(x, dpp<0x4E>(x));
    x = umax(x, dpp<0x141>(x));
    x = umax(x, dpp<0x140>(x));
    auto r = __builtin_amdgcn_permlane16_swap(x, x, false, false);
    return umax(r[0], r[1]);
}
__device__ __forceinline__ f4 mfma(v4u a, v4u b, f4 c) {
    return __builtin_amdgcn_mfma_f32_16x16x32_bf16(__builtin_bit_cast(bf8, a), __builtin_bit_cast(bf8, b), c, 0, 0, 0);
}

#define STAMP(i) do { if ((i) == PROBE_A || (i) == PROBE_B) { SB(); ts[i] = (u32)__builtin_amdgcn_s_memrealtime(); SB(); } } while (0)

__shared__ __attribute__((aligned(16))) char lds_raw[L_END];

__device__ __forceinline__ LAS char* L(int off) { return (LAS char*)lds_raw + off; }
__device__ __forceinline__ void cset(int w, u32 v = 1u) { *(volatile LAS u32*)L(L_C + 4 * w) = v; }
template <int SL = 1> __device__ __forceinline__ u32 cwait(int w, u32 want = 1u) {
    u32 x = 0;
    NOUNROLL
    for (u32 n = 0; n < (1u << 22); ++n) {
        x = __builtin_amdgcn_readfirstlane(*(volatile LAS u32*)L(L_C + 4 * w));
        if (x >= want) break;
        if (SL) __builtin_amdgcn_s_sleep(SL);
    }
    FENCE();
    return x;
}
__device__ __forceinline__ u32 cpeek(int w) { return *(volatile LAS u32*)L(L_C + 4 * w); }
__device__ __forceinline__ void cadd(int w, u32 v = 1u) {
    FENCE();
    if ((threadIdx.x & 63) == 0) __hip_atomic_fetch_add((LAS u32*)L(L_C + 4 * w), v, __ATOMIC_RELAXED, __HIP_MEMORY_SCOPE_WORKGROUP);
}
template <int SL = 1> __device__ __forceinline__ void cwait_eq(int w, u32 mask, u32 want) {
    NOUNROLL
    for (u32 n = 0; n < (1u << 22); ++n) {
        if ((__builtin_amdgcn_readfirstlane(*(volatile LAS u32*)L(L_C + 4 * w)) & mask) == want) break;
        if (SL) __builtin_amdgcn_s_sleep(SL);
    }
    FENCE();
}
__device__ __forceinline__ void wait_flags(rsrc_t rw, int base, u32 tlo, u32 thi, int lane) {
    NOUNROLL
    for (u32 n = 0; n < SPIN_CAP; ++n) {
        FENCE();
        const v4u f0 = ld128<SC1>(rw, lane * 16, base), f1 = ld128<SC1>(rw, lane * 16 + 1024, base);
        if (__all((((f0.x ^ tlo) | (f0.z ^ tlo) | (f1.x ^ tlo) | (f1.z ^ tlo)) | ((f0.y ^ thi) | (f0.w ^ thi) | (f1.y ^ thi) | (f1.w ^ thi))) == 0u)) break;
        __builtin_amdgcn_s_sleep(8);
    }
    FENCE();
}
__device__ __forceinline__ void wait_flag(rsrc_t rw, int off, u32 tlo, u32 thi) {
    NOUNROLL
    for (u32 n = 0; n < SPIN_CAP; ++n) {
        FENCE();
        const v2u f = ld64<SC1>(rw, 0, off);
        if (f.x == tlo && f.y == thi) break;
        __builtin_amdgcn_s_sleep(4);
    }
    FENCE();
}
template <int SL> __device__ __forceinline__ void wait_word(rsrc_t rw, u32 off) {
    NOUNROLL
    for (u32 n = 0; n < SPIN_CAP; ++n) {
        FENCE();
        if (ld32<SC1>(rw, 0, off) != SENT) break;
        __builtin_amdgcn_s_sleep(SL);
    }
    FENCE();
}
template <int N> __device__ __forceinline__ void poll_issue(rsrc_t rw, const u32 (&vo)[N], u32 so, v4u (&v)[N]) {
    FENCE();
#pragma unroll
    for (int i = 0; i < N; ++i) v[i] = ld128<SC1>(rw, vo[i], so);
    SB();
}
template <int N> __device__ __forceinline__ void poll_finish(rsrc_t rw, const u32 (&vo)[N], u32 so, v4u (&v)[N]) {
    NOUNROLL
    for (u32 n = 0; n < SPIN_CAP; ++n) {
        bool ok[N], all = true;
#pragma unroll
        for (int i = 0; i < N; ++i) { ok[i] = valid4(v[i]); all &= ok[i]; }
        if (__all(all)) break;
        FENCE();
#pragma unroll
        for (int i = 0; i < N; ++i) if (!ok[i]) v[i] = ld128<SC1>(rw, vo[i], so);
    }
    FENCE();
}
template <int N> __device__ __forceinline__ void poll16(rsrc_t rw, const u32 (&vo)[N], u32 so, v4u (&v)[N]) {
    poll_issue<N>(rw, vo, so, v);
    poll_finish<N>(rw, vo, so, v);
}
__device__ __forceinline__ u32 selmask(int b) {
    const int lane = threadIdx.x & 63;
    const u32 rel = (2047u - (*(LAS u32*)L(L_SEL + (lane & 7) * 8) & 0xFFFFu)) - 8u * b;
    u32 m = rel < 8u ? 1u << rel : 0u;
    m |= dpp<0xB1>(m);
    m |= dpp<0x4E>(m);
    m |= dpp<0x141>(m);
    return m;
}

#define DSR64(dst, addr, off) asm volatile("ds_read_b64 %0, %1 offset:%2" : "=v"(dst) : "v"(addr), "i"(off))
#define DSR128(dst, addr, off) asm volatile("ds_read_b128 %0, %1 offset:%2" : "=v"(dst) : "v"(addr), "i"(off))
#define MFMA_IP(acc, a, b) asm volatile("v_mfma_f32_16x16x32_bf16 %0, %1, %2, %0" : "+v"(acc) : "v"(a), "v"(b))

struct AF { v2u lo, hi; };
__device__ __forceinline__ v4u cat(const AF& a) { return v4u{a.lo.x, a.lo.y, a.hi.x, a.hi.y}; }

template <int I0> __device__ __forceinline__ void conv_phase(u32 va, u32 vb, f4 (&acc)[8], AF (&alo)[4], v4u (&blo)[4], AF (&alo2)[4], v4u (&blo2)[4]) {
    AF ahi[4];
    v4u bhi[4];
    constexpr int OA = 64 * I0, OB = 448 - 64 * I0;
    asm volatile("s_waitcnt lgkmcnt(0)" : "+v"(alo[0].lo), "+v"(alo[0].hi), "+v"(alo[1].lo), "+v"(alo[1].hi), "+v"(alo[2].lo), "+v"(alo[2].hi), "+v"(alo[3].lo),
                 "+v"(alo[3].hi), "+v"(blo[0]), "+v"(blo[1]), "+v"(blo[2]), "+v"(blo[3]));
    SB();
#pragma unroll
    for (int n = 0; n < 16; ++n) {
        const int e = n >> 2, b = n & 3;
        MFMA_IP(acc[e + b], cat(alo[e]), blo[b]);
        if (n < 4) DSR128(bhi[n], vb, OB + 512 * (4 + n));
        else if (n < 12) {
            if (n & 1) DSR64(ahi[(n - 4) >> 1].hi, va, OA + 512 * (4 + ((n - 4) >> 1)) + 8);
            else DSR64(ahi[(n - 4) >> 1].lo, va, OA + 512 * (4 + ((n - 4) >> 1)));
        }
        SB();
    }
    asm volatile("s_waitcnt lgkmcnt(0)" : "+v"(ahi[0].lo), "+v"(ahi[0].hi), "+v"(ahi[1].lo), "+v"(ahi[1].hi), "+v"(ahi[2].lo), "+v"(ahi[2].hi), "+v"(ahi[3].lo),
                 "+v"(ahi[3].hi), "+v"(bhi[0]), "+v"(bhi[1]), "+v"(bhi[2]), "+v"(bhi[3]));
    SB();
    int n = 0;
#pragma unroll
    for (int half = 0; half < 2; ++half)
#pragma unroll
        for (int e = 0; e < 4; ++e)
#pragma unroll
            for (int b = 0; b < 4 - e; ++b) {
                if (half == 0) MFMA_IP(acc[4 + e + b], cat(alo[e]), bhi[b]);
                else MFMA_IP(acc[4 + e + b], cat(ahi[e]), blo[b]);
                if (I0 < 7) {
                    if (n < 4) DSR128(blo2[n], vb, OB - 64 + 512 * n);
                    else if (n < 12) {
                        if (n & 1) DSR64(alo2[(n - 4) >> 1].hi, va, OA + 64 + 512 * ((n - 4) >> 1) + 8);
                        else DSR64(alo2[(n - 4) >> 1].lo, va, OA + 64 + 512 * ((n - 4) >> 1));
                    }
                }
                ++n;
                SB();
            }
}

__device__ __forceinline__ u32 pf_off(u32 idx, u32 ch, int xq) {
    const u32 g = idx / 36u, rest = idx - 36u * g;
    const u32 row = 64u * (rest >> 2) + 8u * (rest & 3u) + (g < 3u ? 5u + g : 29u + g);
    return row * (F * 2) + xq * 1152 + ch * 16;
}

template <class TS>
__device__ __forceinline__ void ln_part(rsrc_t rw, int b, int lw, int lane, const u32 (&xq9)[11], const u32 (&wr9)[9], TS& ts) {
    const int hf = lane >> 5, l32 = lane & 31, tl = 2 * lw + hf, tok = 8 * b + tl;
    u32 ko = l32 == 0 ? W_KEY + b * 64 + tl * 8 : OOB;
    asm volatile("" : "+v"(ko));
    asm volatile("" ::"v"(xq9[8]));
    STAMP(10);
    f2 x[9];
    f2 s2 = {0.f, 0.f};
#pragma unroll
    for (int i = 0; i < 9; ++i) { x[i] = unpk(xq9[i]); s2 += x[i]; }
    const float mean = sum32(s2.x + s2.y) * (1.0f / D);
    f2 v2 = {0.f, 0.f};
#pragma unroll
    for (int i = 0; i < 9; ++i) { const f2 dl = x[i] - mean; v2 += dl * dl; }
    const u32 clr = cpeek(C_CLR);
    const float var = rbf(sum32(v2.x + v2.y) * (1.0f / D));
    const float mb = rbf(mean), sd = rbf(__builtin_amdgcn_sqrtf(rbf(var + 1e-5f)));
    const float inv = __builtin_amdgcn_rcpf(sd);
    float a0 = 0.f, a1 = 0.f;
#pragma unroll
    for (int i = 0; i < 9; ++i) {
        const u32 xn = pk2((x[i] - mb) * inv);
        if (i & 1) a1 = __builtin_amdgcn_fdot2_f32_bf16(__builtin_bit_cast(bf2, xn), __builtin_bit_cast(bf2, wr9[i]), a1, false);
        else a0 = __builtin_amdgcn_fdot2_f32_bf16(__builtin_bit_cast(bf2, xn), __builtin_bit_cast(bf2, wr9[i]), a0, false);
    }
    const float rs = sum32(a0 + a1);
    const u32 rb = pk(rs, rs) & 0xFFFFu;
    u32 ord = (rb & 0x8000u) ? (~rb & 0xFFFFu) : (rb | 0x8000u);
    if ((rb & 0x7FFFu) > 0x7F80u) ord = 0u;
    v2u rec = {(ord << 16) | (u32)(2047 - tok), nos(pk(mb, sd))};
    asm volatile("" : "+v"(rec));
    STAMP(1);
    if (!__builtin_amdgcn_readfirstlane(clr)) cwait(C_CLR);
    st64<SC1>(rec, rw, ko, 0);
    if (l32 == 0) *(LAS f2*)L(L_STAT + tl * 8) = f2{mb, inv};
    cadd(C_GO);
    STAMP(2);
}

typedef u32 v16u __attribute__((ext_vector_type(16)));
typedef u32 v8u __attribute__((ext_vector_type(8)));
__device__ __forceinline__ u64 mk64(u32 lo, u32 hi) { return ((u64)hi << 32) | lo; }

__global__ void __launch_bounds__(NT, 3) fused_kernel(Params) {
    v16u ka;
    v8u kb;
    asm volatile("s_load_dwordx16 %0, %2, 0x0\n\ts_load_dwordx8 %1, %2, 0x40" : "=&s"(ka), "=&s"(kb) : "s"(__builtin_amdgcn_kernarg_segment_ptr()));
    const int tid = threadIdx.x, lane = tid & 63, wave = __builtin_amdgcn_readfirstlane(tid >> 6);
    const int wg = blockIdx.x, xq = wg & 7, xi = wg >> 3, b = 32 * xq + xi;
    const bool isConv = xi < 18, isMoe = xi >= 18 && xi < 27, isSel = xi == 27;
    const int cj = 18 * xq + xi, mu = 9 * xq + xi - 18;
    const u32 xs = W_XS + xq * XSB;
    u32 ts[12];
#pragma unroll
    for (int i = 0; i < 12; ++i) ts[i] = 0;
    u64 pc;
    asm volatile("s_getpc_b64 %0" : "=s"(pc));
    STAMP(0);
    const u64 pad0 = __builtin_amdgcn_s_memrealtime();
    const bool isK = wave >= 1 && wave < 6, isL = wave >= 6;
    const int cp = tid - 64;
    const int lw = wave - 6;
    const int mw = wave - 1;
    u32 so[12], nsk = isK ? D * 8 : 0, nv = isK ? S * D * 2 : (isL ? 384 + D * 2 : 0);
    {
        u32 s0 = isL ? 0u : (u32)(8 * b - 3) * 1152u, st = isL ? 128u : 1152u;
        asm volatile("" : "+s"(s0), "+s"(st), "+s"(nsk), "+s"(nv));
#pragma unroll
        for (int i = 0; i < 12; ++i) {
            so[i] = s0 + i * st;
            asm volatile("" : "+s"(so[i]));
        }
    }
    u32 va0 = (u32)cp < 288u ? cp * 4 : OOB, va1 = (8 * b + 2 * lw + (lane >> 5)) * 1152 + (lane & 31) * 4;
    asm volatile("" : "+v"(va0), "+v"(va1));
    u32 vb0 = tid >= 384 ? (lane & 31) * 4 : va0, vb1 = tid >= 384 ? (lane & 31) * 4 : OOB;
    va0 = tid >= 384 ? va1 : va0;
    asm volatile("" : "+v"(va0), "+v"(vb0), "+v"(vb1));
    SB();
    v4u pf = {0u, 0u, 0u, 0u};
    if (isL) {
        if (CODE_KB && xi * 4 + lw < CODE_KB) pf = *(const GAS v4u*)((pc & ~255ull) + (xi * 4 + lw) * 1024 + lane * 16);
        if (Q_DELAY) __builtin_amdgcn_s_sleep(Q_DELAY);
    }
    SB();
    asm volatile("s_waitcnt lgkmcnt(0)" : "+s"(ka), "+s"(kb));
    Params p;
    p.q = (const u16*)mk64(ka[0], ka[1]);
    p.k = (const u16*)mk64(ka[2], ka[3]);
    p.v = (const u16*)mk64(ka[4], ka[5]);
    p.sk = (const u16*)mk64(ka[6], ka[7]);
    p.h = (const u16*)mk64(ka[8], ka[9]);
    p.dv = (const u16*)mk64(ka[10], ka[11]);
    p.wr = (const u16*)mk64(ka[12], ka[13]);
    p.wg = (const u16*)mk64(ka[14], ka[15]);
    p.wu = (const u16*)mk64(kb[0], kb[1]);
    p.wd = (const u16*)mk64(kb[2], kb[3]);
    p.out = (u16*)mk64(kb[4], kb[5]);
    p.ws = (char*)mk64(kb[6], kb[7]);
    const rsrc_t rw = mkrs(p.ws, WS_BYTES);
    const rsrc_t rq = mkrs(p.q, S * D * 2);

    u64 pa = isL ? (u64)p.q : (u64)p.k, pb = isL ? (u64)p.wr - 384 : (u64)p.v;
    asm volatile("" : "+s"(pa), "+s"(pb));
    const v4u s4 = {SENT, SENT, SENT, SENT};
    u32 ra[11], rb[9];
    v4u sk;
    if (wave != 0) {
        sk = ld128<0>(mkrs(p.sk, nsk), cp * 16, 0);
        const rsrc_t da = mkrs((const void*)pa, S * D * 2), db = mkrs((const void*)pb, nv);
#pragma unroll
        for (int i = 0; i < 11; ++i) {
            ra[i] = ld32<AUX_IN>(da, va0, so[i]);
            if (i >= 3) rb[i - 3] = ld32<AUX_IN>(db, vb0, so[i]);
        }
        rb[8] = ld32<AUX_IN>(db, vb1, so[11]);
    }
    SB();
    v2u e = {0u, 0u};
    if (wave == 0) {
#pragma unroll
        for (int i = 0; i < 9; ++i) st128<SC1>(s4, rw, lane * 16 + 1024 * i, W_U + b * 9216);
        st128<SC1>(s4, rw, lane < 4 ? W_KEY + b * 64 + lane * 16 : OOB, 0);
        e = ld64<SC1>(rw, 0, W_E);
        if (lane < 16) *(LAS u32*)L(L_C + 4 * lane) = 0u;
    }
    STAMP(11);
    bar();
    STAMP(8);

    if (wave == 0) {
        DRAIN();
        const u64 tag = (((u64)e.y << 32) | e.x) + 1ull;
        const u32 tlo = __builtin_amdgcn_readfirstlane((u32)tag), thi = __builtin_amdgcn_readfirstlane((u32)(tag >> 32));
        if (lane == 0) st64<SC1>(v2u{tlo, thi}, rw, 0, W_F1 + wg * 8);
        cset(C_TLO, tlo);
        cset(C_THI, thi);
        cset(C_CLR);
        STAMP(1);
        if (isMoe) {
#pragma unroll
            for (int i = 0; i < 18; ++i) st128<SC1>(s4, rw, lane * 16 + 1024 * i, W_PART + mu * 18432);
            cadd(C_GO, 0x100u);
            DRAIN();
            if (lane == 0) st64<SC1>(v2u{tlo, thi}, rw, 0, W_F2 + wg * 8);
            STAMP(2);
            bar();
            bar();
            {
                const v4u sd = {(u32)(u64)p.wd, (u32)((u64)p.wd >> 32) & 0xFFFFu, (u32)(F * D * 2), 0x00020000u};
                u32 ro = 64 * mu * 1152;
                asm volatile("s_mov_b32 m0, %2\n\ts_nop 0\n\t.rept 64\n\tbuffer_load_dwordx4 %1, %3, %0 offen lds\n\ts_add_u32 m0, m0, 1056\n\t"
                             "s_add_u32 %0, %0, 1152\n\t.endr"
                             : "+s"(ro) : "v"(lane * 16), "s"((u32)(uintptr_t)L(L_DN)), "s"(sd) : "memory", "scc");
                ro = 64 * mu * 1152;
                asm volatile("s_mov_b32 m0, %2\n\ts_nop 0\n\t.rept 8\n\tbuffer_load_dwordx4 %1, %3, %0 offen lds\n\ts_add_u32 m0, m0, 1024\n\t"
                             "s_add_u32 %0, %0, 9216\n\t.endr"
                             : "+s"(ro) : "v"((lane >> 3) * 1152 + 1024 + (lane & 7) * 16), "s"((u32)(uintptr_t)L(L_DT)), "s"(sd) : "memory", "scc");
                STAMP(3);
                DRAIN();
                STAMP(4);
            }
            bar();
            bar();
            u32 vo[1] = {(u32)(lane & 3) * 16};
            v4u sv[1];
            poll16<1>(rw, vo, xs, sv);
            if (lane < 4) *(LAS v4u*)L(L_SEL + lane * 16) = sv[0];
            LGKM0();
            cset(C_SEL, 0x100u | selmask(b));
            wait_flags(rw, W_F2, tlo, thi, lane);
            cset(C_F2);
        } else if (isSel) {
#pragma unroll
            for (int i = 0; i < 9; ++i) st128<SC1>(s4, rw, lane * 16 + 1024 * i, xs + 128);
            st128<SC1>(s4, rw, lane < 4 ? xs + lane * 16 : OOB, 0);
            DRAIN();
            if (lane == 0) {
                st64<SC1>(v2u{tlo, thi}, rw, 0, W_F2 + wg * 8);
                st64<SC1>(v2u{tlo, thi}, rw, 0, W_F3 + xq * 128);
            }
            cwait_eq(C_GO, 0xFu, 4u);
            if (K_DELAY) __builtin_amdgcn_s_sleep(K_DELAY);
            wait_flags(rw, W_F1, tlo, thi, lane);
            STAMP(2);
            bar();
            if (xq == 0 && lane == 0) st64<SC1>(v2u{tlo, thi}, rw, 0, W_E);
            bar();
            bar();
            bar();
            cset(C_SEL, 0x100u | selmask(b));
            __builtin_amdgcn_s_sleep(32);
            wait_flags(rw, W_F2, tlo, thi, lane);
            cset(C_F2);
        } else {
            if (isConv) {
                if (F_DELAY) __builtin_amdgcn_s_sleep(F_DELAY);
                wait_flags(rw, W_F1, tlo, thi, lane);
                cset(C_F1);
                STAMP(2);
#pragma unroll
                for (int i = 0; i < 16; ++i) st128<SC1>(s4, rw, lane * 16 + 1024 * i, W_Y + cj * 16384);
                bar();
                DRAIN();
                cadd(C_YL);
                if (lane == 0) st64<SC1>(v2u{tlo, thi}, rw, 0, W_F2 + wg * 8);
            } else {
                if (lane == 0) st64<SC1>(v2u{tlo, thi}, rw, 0, W_F2 + wg * 8);
                cwait<4>(C_GO, 0x54u);
                __builtin_amdgcn_s_sleep(32);
            }
            wait_flag(rw, W_F3 + xq * 128, tlo, thi);
            STAMP(3);
            u32 vo[1] = {(u32)(lane & 3) * 16};
            v4u sv[1];
            wait_word<16>(rw, xs + 60);
            poll16<1>(rw, vo, xs, sv);
            if (lane < 4) *(LAS v4u*)L(L_SEL + lane * 16) = sv[0];
            LGKM0();
            cset(C_SEL, 0x100u | selmask(b));
            STAMP(4);
            wait_flags(rw, W_F2, tlo, thi, lane);
            cset(C_F2);
            STAMP(5);
            if (isConv) {
                const int s = cj / 18, cb = cj % 18, d = 32 * cb + (lane & 31);
                const v2u me = *(LAS v2u*)L(L_SEL + s * 8);
                const int tok = (2047 - me.x) & 2047;
                const u32 ord = me.x >> 16, rb = (ord & 0x8000u) ? (ord & 0x7FFFu) : (~ord & 0xFFFFu);
                const float mb = lo16(me.y), inv = __builtin_amdgcn_rcpf(hi16(me.y));
                const u32 eo = (tok * D + d) * 2;
                const rsrc_t rk = mkrs(p.k, S * D * 2);
                const u32 xb = __builtin_amdgcn_raw_buffer_load_b16(rq, eo, 0, 0), vb = __builtin_amdgcn_raw_buffer_load_b16(mkrs(p.v, S * D * 2), eo, 0, 0);
                u32 kb[4];
#pragma unroll
                for (int j = 0; j < 4; ++j) kb[j] = __builtin_amdgcn_raw_buffer_load_b16(rk, tok >= j ? eo - j * 1152 : OOB, 0, 0);
                const v2u sw = ld64<0>(mkrs(p.sk, D * 8), d * 8, 0);
                const u32 db = __builtin_amdgcn_raw_buffer_load_b16(mkrs(p.dv, D * 2), d * 2, 0, 0);
                const float x = lo16(xb);
                const float kv = rbf((lo16(kb[3]) * hi16(sw.y) + lo16(kb[2]) * lo16(sw.y) + lo16(kb[1]) * hi16(sw.x) + lo16(kb[0]) * lo16(sw.x)) * lo16(vb));
                const float xn = rbf((x - mb) * inv);
                float base = x + xn * kv * lo16(db);
                float rsg = rbf(__builtin_amdgcn_rcpf(1.0f + __expf(-lo16(rb))));
                const int c = (d & 1) * 288 + (d >> 1);
                u32 yo = (c >> 2) * 16384 + (c & 3) * 16 + (tok >> 3) * 64 + ((tok & 7) >> 1) * 4;
                u32 vp[9];
#pragma unroll
                for (int i = 0; i < 9; ++i) vp[i] = (8 * i + (lane >> 3)) * 18432 + (lane & 7) * 16;
                asm volatile("" : "+v"(base), "+v"(rsg), "+v"(yo));
                wait_word<4>(rw, W_PART + (8 * (cj % 9) + 3) * 18432 + s * 2304 + cb * 128);
                cwait<2>(C_YST, 4);
                if (Y_DELAY) __builtin_amdgcn_s_sleep(Y_DELAY);
                STAMP(6);
                v4u pv[9];
                u32 yw = ld32<SC1>(rw, yo, W_Y);
                poll16<9>(rw, vp, W_PART + s * 2304 + cb * 128, pv);
                STAMP(7);
                f4 a = {0.f, 0.f, 0.f, 0.f};
#pragma unroll
                for (int i = 0; i < 9; ++i) a += __builtin_bit_cast(f4, pv[i]);
#pragma unroll
                for (int i = 0; i < 4; ++i) {
                    float t = a[i];
                    t += asf(dpp<0x128>(asu(t)));
                    const auto r1 = __builtin_amdgcn_permlane16_swap(asu(t), asu(t), false, false);
                    t = asf(r1[0]) + asf(r1[1]);
                    const auto r2 = __builtin_amdgcn_permlane32_swap(asu(t), asu(t), false, false);
                    a[i] = asf(r2[0]) + asf(r2[1]);
                }
                if (lane < 8) *(LAS f4*)L(L_RED + lane * 16) = a;
                LGKM0();
                base += rbf(xn + rbf(rbf(*(LAS float*)L(L_RED + (lane & 31) * 4)) * rsg));
                asm volatile("" : "+v"(base));
                STAMP(8);
                NOUNROLL
                for (u32 n = 0; n < SPIN_CAP; ++n) {
                    if (__all(yw != SENT)) break;
                    FENCE();
                    yw = ld32<SC1>(rw, yo, W_Y);
                }
                const float o = base + xn * ((tok & 1) ? hi16(yw) : lo16(yw));
                if (lane < 32) __builtin_amdgcn_raw_buffer_store_b16((u16)pk(o, 0.f), mkrs(p.out, S * D * 2), eo, 0, AUX_OUT);
                STAMP(9);
            }
        }
    } else {
        if (isK) {
            u32 uo = cp < 288 ? W_U + b * 9216 + cp * 16 : OOB;
            u32 pfo[2];
#pragma unroll
            for (int i = 0; i < 2; ++i) pfo[i] = cp < 288 ? pf_off(4 * (18 * i + xi) + (u32)cp / 72, (u32)cp % 72, xq) : OOB;
            asm volatile("" : "+v"(uo), "+v"(pfo[0]), "+v"(pfo[1]));
            const f2 w0 = {lo16(sk.x), lo16(sk.z)}, w1 = {hi16(sk.x), hi16(sk.z)}, w2 = {lo16(sk.y), lo16(sk.w)}, w3 = {hi16(sk.y), hi16(sk.w)};
            f2 kf[11], r[8];
            asm volatile("" ::"v"(rb[7]));
            STAMP(10);
            SB();
            v4u hd[4];
            {
                const int c = 4 * cj + mw;
                const rsrc_t rh = mkrs(p.h, isConv && mw < 4 ? D * S * 2 : 0);
#pragma unroll
                for (int i = 0; i < 4; ++i) hd[i] = ld128<AUX_IN>(rh, lane * 16 + 1024 * i, (c < 288 ? 2 * c : 2 * (c - 288) + 1) * 4096);
            }
            v4u t1[4] = {s4, s4, s4, s4};
            if (PF1 == 1) {
                const rsrc_t rg = mkrs(p.wg, isConv ? D * F * 2 : 0), ru = mkrs(p.wu, isConv ? D * F * 2 : 0);
#pragma unroll
                for (int i = 0; i < 2; ++i) {
                    t1[2 * i] = ld128<0>(rg, pfo[i], 0);
                    t1[2 * i + 1] = ld128<0>(ru, pfo[i], 0);
                }
            }
            SB();
#pragma unroll
            for (int i = 0; i < 11; ++i) kf[i] = unpk(ra[i]);
#pragma unroll
            for (int t = 0; t < 8; ++t) r[t] = (kf[t] * w3 + kf[t + 1] * w2 + kf[t + 2] * w1 + kf[t + 3] * w0) * unpk(rb[t]);
            const u32 clr = cpeek(C_CLR);
            v4u c0 = v4u{nos(pk(r[7].x, r[6].x)), nos(pk(r[5].x, r[4].x)), nos(pk(r[3].x, r[2].x)), nos(pk(r[1].x, r[0].x))};
            v4u c1 = v4u{nos(pk(r[7].y, r[6].y)), nos(pk(r[5].y, r[4].y)), nos(pk(r[3].y, r[2].y)), nos(pk(r[1].y, r[0].y))};
            asm volatile("" : "+v"(c0), "+v"(c1));
            STAMP(1);
            if (!__builtin_amdgcn_readfirstlane(clr)) cwait(C_CLR);
            st128<SC1>(c0, rw, uo, 0);
            st128<SC1>(c1, rw, uo + 4608, 0);
            cadd(C_GO, 0x10u);
            STAMP(2);
            if (PF1 == 2) {
                const rsrc_t rg = mkrs(p.wg, isConv ? D * F * 2 : 0), ru = mkrs(p.wu, isConv ? D * F * 2 : 0);
#pragma unroll
                for (int i = 0; i < 2; ++i) {
                    t1[2 * i] = ld128<0>(rg, pfo[i], 0);
                    t1[2 * i + 1] = ld128<0>(ru, pfo[i], 0);
                }
            }
            *(LAS v4u*)L(L_ST + cp * 16) = c0;
            *(LAS v4u*)L(L_ST + 5120 + cp * 16) = c1;
            if (isConv && mw < 4) {
                LAS char* hb = L(L_H + mw * HCH);
#pragma unroll
            for (int i = 0; i < 4; ++i) {
                const v4u dd = hd[i];
                const u32 e0 = i < 3 ? __builtin_amdgcn_readfirstlane(hd[i < 3 ? i + 1 : 3].x) : 0u;
                const u32 e1 = i < 3 ? __builtin_amdgcn_readfirstlane(hd[i < 3 ? i + 1 : 3].y) : 0u;
                const u32 n0 = __builtin_amdgcn_update_dpp(e0, dd.x, 0x130, 0xF, 0xF, false);
                const u32 n1 = __builtin_amdgcn_update_dpp(e1, dd.y, 0x130, 0xF, 0xF, false);
                const u32 a01 = __builtin_amdgcn_alignbit(dd.y, dd.x, 16), a12 = __builtin_amdgcn_alignbit(dd.z, dd.y, 16),
                          a23 = __builtin_amdgcn_alignbit(dd.w, dd.z, 16), a3n = __builtin_amdgcn_alignbit(n0, dd.w, 16),
                          ann = __builtin_amdgcn_alignbit(n1, n0, 16);
                LAS char* o = hb + (lane + 64 * i + 2) * 16;
                *(LAS v4u*)(o) = dd;
                *(LAS v4u*)(o + HC) = v4u{a01, a12, a23, a3n};
                *(LAS v4u*)(o + 2 * HC) = v4u{dd.y, dd.z, dd.w, n0};
                *(LAS v4u*)(o + 3 * HC) = v4u{a12, a23, a3n, ann};
            }
            {
                const u32 h0 = __builtin_amdgcn_readfirstlane(hd[0].x), h1 = __builtin_amdgcn_readfirstlane(hd[0].y);
                if (lane < 8) {
                    const bool od = lane & 1;
                    const int sg = lane >> 1;
                    v4u z = {0u, 0u, 0u, 0u};
                    z.z = od && sg == 3 ? h0 << 16 : 0u;
                    z.w = !od || sg == 0 ? 0u : (sg == 1 ? h0 << 16 : (sg == 2 ? h0 : (h0 >> 16) | (h1 << 16)));
                    *(LAS v4u*)(hb + sg * HC + (lane & 1) * 16) = z;
                }
            }
                STAMP(7);
            }
            asm volatile("" ::"v"(t1[0]), "v"(t1[1]), "v"(t1[2]), "v"(t1[3]));
            if (isSel) {
                *(LAS v4u*)L(L_LK + cp * 16) = v4u{0u, 0u, 0u, 0u};
                if (cp < 196) *(LAS v4u*)L(L_LK + 5120 + cp * 16) = v4u{0u, 0u, 0u, 0u};
            }
        } else if (isConv) {
            LAS char* hb = L(L_H + lw * HCH);
            const int l = tid - 384;
            if (lane < 32) *(LAS v4u*)L(L_U + lw * UCH + lane * 16) = v4u{0u, 0u, 0u, 0u};
            f4 acc[8];
#pragma unroll
            for (int i = 0; i < 8; ++i) {
                acc[i] = f4{0.f, 0.f, 0.f, 0.f};
                asm volatile("" : "+v"(acc[i]));
            }
            u32 vo[4];
#pragma unroll
            for (int i = 0; i < 4; ++i) vo[i] = (64 * i + (l >> 2)) * 9216 + (l & 3) * 16;
            const int m1 = (lane & 15) + 1, kq = lane >> 4;
            u32 va = (u32)(uintptr_t)(hb + (m1 & 3) * HC + 16 * kq + 8 * (m1 >> 2));
            u32 vb = (u32)(uintptr_t)L(L_U + lw * UCH + (33 + 2 * (lane & 15) - kq) * 16 - 448);
            u32 uw = (u32)(uintptr_t)L(L_U + (l & 3) * UCH + (32 + (l >> 2)) * 16);
            u32 yw = (u32)(uintptr_t)L(L_Y + (32 * (kq & 1) + 2 * (lane & 15) + (kq >> 1)) * 64 + ((lw ^ lane) & 3) * 16);
            u32 yr = (u32)(uintptr_t)L(L_Y + (l >> 2) * 64 + ((l ^ (l >> 3)) & 3) * 16);
            u32 yg = W_Y + cj * 16384 + l * 16;
            asm volatile("" : "+v"(vo[0]), "+v"(vo[1]), "+v"(vo[2]), "+v"(vo[3]), "+v"(va), "+v"(vb), "+v"(uw), "+v"(yw), "+v"(yr), "+v"(yg));
            SB();
            STAMP(3);
            ln_part(rw, b, lw, lane, ra, rb, ts);
            asm volatile("" ::"v"(pf));
            cwait_eq(C_GO, 0xF0u, 0x50u);
            if (U_DELAY) __builtin_amdgcn_s_sleep(U_DELAY);
            cwait(C_F1);
            STAMP(4);
            v4u uv[4];
            poll_issue<4>(rw, vo, W_U + cj * 64, uv);
            poll_finish<4>(rw, vo, W_U + cj * 64, uv);
#pragma unroll
            for (int i = 0; i < 4; ++i) *(LAS v4u*)((LAS char*)(uintptr_t)uw + 1024 * i) = uv[i];
            STAMP(5);
            bar();
            STAMP(8);
            AF a0[4], a1[4];
            v4u b0[4], b1[4];
#pragma unroll
            for (int n = 0; n < 4; ++n) {
                DSR128(b0[n], vb, 448 + 512 * n);
                DSR64(a0[n].lo, va, 512 * n);
                DSR64(a0[n].hi, va, 512 * n + 8);
            }
            conv_phase<0>(va, vb, acc, a0, b0, a1, b1);
            conv_phase<1>(va, vb, acc, a1, b1, a0, b0);
            conv_phase<2>(va, vb, acc, a0, b0, a1, b1);
            conv_phase<3>(va, vb, acc, a1, b1, a0, b0);
            conv_phase<4>(va, vb, acc, a0, b0, a1, b1);
            conv_phase<5>(va, vb, acc, a1, b1, a0, b0);
            conv_phase<6>(va, vb, acc, a0, b0, a1, b1);
            conv_phase<7>(va, vb, acc, a1, b1, a0, b0);
            asm volatile("s_nop 7\n\ts_nop 1" : "+v"(acc[0]), "+v"(acc[1]), "+v"(acc[2]), "+v"(acc[3]), "+v"(acc[4]), "+v"(acc[5]), "+v"(acc[6]), "+v"(acc[7]));
            STAMP(6);
#pragma unroll
            for (int q = 0; q < 8; q += 2) {
                const u32 y0 = nos(pk(acc[q][0], acc[q][1])), y1 = nos(pk(acc[q][2], acc[q][3]));
                const u32 z0 = nos(pk(acc[q + 1][0], acc[q + 1][1])), z1 = nos(pk(acc[q + 1][2], acc[q + 1][3]));
                const auto s0 = __builtin_amdgcn_permlane16_swap(y0, z0, false, false);
                const auto s1 = __builtin_amdgcn_permlane16_swap(y1, z1, false, false);
                *(LAS v4u*)(uintptr_t)(yw + q * 2048) = v4u{s0[0], s1[0], s0[1], s1[1]};
            }
            cadd(C_YL);
            cwait<0>(C_YL, 5);
            {
                v4u yv[4];
#pragma unroll
                for (int i = 0; i < 4; ++i) yv[i] = *(LAS v4u*)(uintptr_t)(yr + 4096 * i);
#pragma unroll
                for (int i = 0; i < 4; ++i) st128<SC1>(yv[i], rw, yg + 4096 * i, 0);
            }
            cadd(C_YST);
            STAMP(7);
        } else {
            ln_part(rw, b, lw, lane, ra, rb, ts);
            asm volatile("" ::"v"(pf));
        }

        if (isMoe) {
            const int c = lane & 15, kq = lane >> 4;
            const GAS char* gp = (const GAS char*)(c < 8 ? p.wg : p.wu) + (size_t)(64 * mw + 8 * kq) * (F * 2) + mu * 128 + (c & 7) * 16;
            v4u gr[2][8];
            cwait(C_GO, 0x154u);
#pragma unroll
            for (int e = 0; e < 8; ++e) gr[0][e] = *(const GAS v4u*)(gp + (size_t)e * (F * 2));
            bar();
#pragma unroll
            for (int e = 0; e < 8; ++e) gr[1][e] = *(const GAS v4u*)(gp + (size_t)(32 + e) * (F * 2));
            bar();
            STAMP(11);
            v4u gf[2][8];
#pragma unroll
            for (int ks = 0; ks < 2; ++ks) {
#pragma unroll
                for (int nn = 0; nn < 8; ++nn)
#pragma unroll
                    for (int pp = 0; pp < 4; ++pp)
                        gf[ks][nn][pp] = __builtin_amdgcn_perm(gr[ks][2 * pp + 1][nn >> 1], gr[ks][2 * pp][nn >> 1], (nn & 1) ? 0x07060302u : 0x05040100u);
#pragma unroll
                for (int nn = 0; nn < 8; ++nn) asm volatile("" : "+v"(gf[ks][nn]));
                SB();
            }
            STAMP(7);
            u32 xo[2] = {128 + (c & 7) * 1152 + (64 * mw + 8 * kq) * 2u, 192 + (c & 7) * 1152 + (64 * mw + 8 * kq) * 2u};
            u32 ro = (u32)(uintptr_t)L(L_R + ((mw * 8 + 4 * (kq & 1)) * 128 + 8 * c) * 4);
            u32 po = W_PART + mu * 18432 + mw * 256 + (c & 7) * 2304 + (c >> 3) * 64 + kq * 16;
            u32 hwo = (u32)(uintptr_t)L(L_HA + mw * HAS + ((lane & 32) + 8 * ((lane & 15) >> 2) + 4 * ((lane >> 4) & 1) + (lane & 3)) * 2);
            asm volatile("" : "+v"(hwo));
            u32 ta[4];
#pragma unroll
            for (int i = 0; i < 4; ++i) {
                const int row = 16 * i + 4 * kq + (c >> 2);
                ta[i] = (u32)(uintptr_t)L(mw < 8 ? L_DN + row * DNS + 128 * mw + 8 * (c & 3) : L_DT + row * 128 + 8 * (c & 3));
                asm volatile("" : "+v"(ta[i]));
            }
            asm volatile("" : "+v"(xo[0]), "+v"(xo[1]), "+v"(ro), "+v"(po));
            STAMP(8);
            if (wave == 9) {
                wait_flag(rw, W_F3 + xq * 128, *(volatile LAS u32*)L(L_C + 4 * C_TLO), *(volatile LAS u32*)L(L_C + 4 * C_THI));
                wait_word<2>(rw, xs + 60);
                cset(C_XS);
            }
            cwait<2>(C_XS);
            if (X_DELAY) __builtin_amdgcn_s_sleep(X_DELAY);
            {
                v4u xa[2];
                STAMP(4);
                poll16<2>(rw, xo, xs, xa);
                STAMP(5);
                const v4u xa0 = xa[0], xa1 = xa[1];
                f4 g[8];
#pragma unroll
                for (int nn = 0; nn < 8; ++nn) g[nn] = mfma(xa1, gf[1][nn], mfma(xa0, gf[0][nn], f4{0.f, 0.f, 0.f, 0.f}));
#pragma unroll
                for (int r = 0; r < 4; ++r) {
                    const f4 o = kq < 2 ? f4{g[0][r], g[1][r], g[2][r], g[3][r]} : f4{g[4][r], g[5][r], g[6][r], g[7][r]};
                    *(LAS f4*)(uintptr_t)(ro + r * 512 + (kq >> 1) * 16) = o;
                }
            }
            bar();
            v2u dl[2][4], dh[2][4];
            {
                float gv[9], uv[9];
#pragma unroll
                for (int w = 0; w < 9; ++w) {
                    gv[w] = *(LAS float*)L(L_R + ((w * 8 + mw) * 128 + lane) * 4);
                    uv[w] = *(LAS float*)L(L_R + ((w * 8 + mw) * 128 + 64 + lane) * 4);
                }
                SB();
                float g = ((gv[0] + gv[1]) + (gv[2] + gv[3])) + ((gv[4] + gv[5]) + (gv[6] + gv[7])) + gv[8];
                float u = ((uv[0] + uv[1]) + (uv[2] + uv[3])) + ((uv[4] + uv[5]) + (uv[6] + uv[7])) + uv[8];
                asm volatile("" : "+v"(g), "+v"(u));
                SB();
#pragma unroll
                for (int ks = 0; ks < 2; ++ks)
#pragma unroll
                    for (int nt = 0; nt < 4; ++nt) {
                        asm volatile("ds_read_b64_tr_b16 %0, %1 offset:%2" : "=v"(dl[ks][nt]) : "v"(ta[2 * ks]), "i"(32 * nt));
                        asm volatile("ds_read_b64_tr_b16 %0, %1 offset:%2" : "=v"(dh[ks][nt]) : "v"(ta[2 * ks + 1]), "i"(32 * nt));
                    }
                SB();
                g = rbf(g);
                u = rbf(u);
                const float sg = rbf(g * __builtin_amdgcn_rcpf(1.0f + __expf(-g)));
                if (mw < 8) *(LAS u16*)(uintptr_t)hwo = (u16)pk(sg * u, 0.f);
            }
            bar();
            asm volatile("" : "+v"(dl[0][0]), "+v"(dl[0][1]), "+v"(dl[0][2]), "+v"(dl[0][3]), "+v"(dl[1][0]), "+v"(dl[1][1]), "+v"(dl[1][2]), "+v"(dl[1][3]),
                         "+v"(dh[0][0]), "+v"(dh[0][1]), "+v"(dh[0][2]), "+v"(dh[0][3]), "+v"(dh[1][0]), "+v"(dh[1][1]), "+v"(dh[1][2]), "+v"(dh[1][3]));
            STAMP(9);
            {
                const v4u ha0 = *(LAS v4u*)L(L_HA + (c & 7) * HAS + 16 * kq), ha1 = *(LAS v4u*)L(L_HA + (c & 7) * HAS + 16 * kq + 64);
                f4 y[4];
#pragma unroll
                for (int nt = 0; nt < 4; ++nt)
                    y[nt] = mfma(v4u{dl[1][nt].x, dl[1][nt].y, dh[1][nt].x, dh[1][nt].y}, ha1,
                                 mfma(v4u{dl[0][nt].x, dl[0][nt].y, dh[0][nt].x, dh[0][nt].y}, ha0, f4{0.f, 0.f, 0.f, 0.f}));
#pragma unroll
                for (int i = 0; i < 2; ++i) {
                    const f4 o = c < 8 ? y[2 * i] : y[2 * i + 1];
                    st128<SC1>(v4u{nos(asu(o[0])), nos(asu(o[1])), nos(asu(o[2])), nos(asu(o[3]))}, rw, po + 128 * i, 0);
                }
            }
            STAMP(10);
        } else if (isSel) {
            const int sw = mw < 4 ? mw : mw - 1;
            u32 vo[2] = {(u32)(sw * 64 + lane) * 16, (u32)(sw * 64 + lane) * 16 + 8192};
            v4u kv2[2];
            bar();
            if (mw != 4) {
                poll16<2>(rw, vo, W_KEY, kv2);
                u32 mx = max32(umax(umax(kv2[0].x, kv2[0].z), umax(kv2[1].x, kv2[1].z)));
                const auto s2 = __builtin_amdgcn_permlane32_swap(mx, mx, false, false);
                if (lane == 0) *(LAS u32*)L(L_WM + 4 * sw) = umax(s2[0], s2[1]);
            }
            STAMP(7);
            bar();
            if (mw != 4) {
                const v4u m0 = *(LAS v4u*)L(L_WM), m1 = *(LAS v4u*)L(L_WM + 16);
                const u32 thr = umin(umin(umin(m0.x, m0.y), umin(m0.z, m0.w)), umin(umin(m1.x, m1.y), umin(m1.z, m1.w)));
                u64 bm[4];
                u32 tot = 0;
#pragma unroll
                for (int i = 0; i < 4; ++i) {
                    bm[i] = __builtin_amdgcn_ballot_w64(((i & 1) ? kv2[i >> 1].z : kv2[i >> 1].x) >= thr);
                    tot += __builtin_popcountll(bm[i]);
                }
                u32 pos = 0;
                if (lane == 0) pos = __hip_atomic_fetch_add((LAS u32*)L(L_C + 4 * C_CNT), tot, __ATOMIC_RELAXED, __HIP_MEMORY_SCOPE_WORKGROUP);
                pos = __builtin_amdgcn_readfirstlane(pos);
#pragma unroll
                for (int i = 0; i < 4; ++i) {
                    const u32 key = (i & 1) ? kv2[i >> 1].z : kv2[i >> 1].x, ms = (i & 1) ? kv2[i >> 1].w : kv2[i >> 1].y;
                    const u32 at = __builtin_amdgcn_mbcnt_hi((u32)(bm[i] >> 32), __builtin_amdgcn_mbcnt_lo((u32)bm[i], pos));
                    const u32 o = key >= thr ? at * 4 : 8272u;
                    *(LAS u32*)L(L_LK + o) = key;
                    *(LAS u32*)L(L_LM + o) = ms;
                    pos += __builtin_popcountll(bm[i]);
                }
            }
            bar();
            {
                const int n = __builtin_amdgcn_readfirstlane(*(LAS u32*)L(L_C + 4 * C_CNT));
                NOUNROLL
                for (int i = tid - 64; i < n; i += 576) {
                    const u32 me = *(LAS u32*)L(L_LK + i * 4), ms = *(LAS u32*)L(L_LM + i * 4);
                    int rank = 0;
                    NOUNROLL
                    for (int j = 0; j < n; j += 32) {
                        v4u t[8];
#pragma unroll
                        for (int u = 0; u < 8; ++u) t[u] = *(LAS v4u*)L(L_LK + (j + 4 * u) * 4);
#pragma unroll
                        for (int u = 0; u < 8; ++u)
#pragma unroll
                            for (int z = 0; z < 4; ++z) rank += t[u][z] > me;
                    }
                    if (rank < 8) *(LAS v2u*)L(L_SEL + rank * 8) = v2u{me, ms};
                }
            }
            const int s = cp / 72, ch = cp - 72 * s;
            bar();
            STAMP(9);
            {
                const v2u me = *(LAS v2u*)L(L_SEL + s * 8);
                const v4u xr = ld128<0>(rq, ((2047 - me.x) & 2047) * 1152 + ch * 16, 0);
                if (cp < 4) st128<SC1>(*(LAS v4u*)L(L_SEL + cp * 16), rw, xs + cp * 16, 0);
                const float mb = lo16(me.y), inv = __builtin_amdgcn_rcpf(hi16(me.y));
                v4u o;
#pragma unroll
                for (int i = 0; i < 4; ++i) o[i] = nos(pk2((unpk(xr[i]) - mb) * inv));
                st128<SC1>(o, rw, xs + 128 + cp * 16, 0);
            }
            STAMP(10);
        } else if (HELP_GU && !isConv) {
            const rsrc_t rg = mkrs(p.wg, D * F * 2), ru = mkrs(p.wu, D * F * 2);
            v4u t[12], t3[2];
            cwait(C_GO, 0x54u);
            if (HELP_C3) {
                const u32 r = ((xi - 28) * 9 + mw) * 8 + (lane >> 3), m = (3u - xq) & 7u;
                const u32 o = (64 * (r >> 5) + (r & 31)) * (F * 2) + (9 * xq + m) * 128 + (lane & 7) * 16;
                t3[0] = ld128<0>(rg, o, 0);
                t3[1] = ld128<0>(ru, o, 0);
            }
#pragma unroll
            for (int i = 0; i < 6; ++i) {
                const u32 idx = ((xi - 28) * 9 + mw) * 64 + lane + 2304 * i, r = idx / 72, ch = idx - 72 * r;
                const u32 o = r < 180 ? pf_off(144 + r, ch, xq) : OOB;
                t[2 * i] = ld128<0>(rg, o, 0);
                t[2 * i + 1] = ld128<0>(ru, o, 0);
            }
            STAMP(7);
#pragma unroll
            for (int i = 0; i < 12; ++i) asm volatile("" ::"v"(t[i]));
            if (HELP_C3) asm volatile("" ::"v"(t3[0]), "v"(t3[1]));
            STAMP(9);
        }

        if (isK) {
            const u32 vo = cp < 288 ? cp * 4 : OOB;
            u32 qq[8];
#pragma unroll
            for (int i = 0; i < 8; ++i) qq[i] = ld32<0>(rq, vo, (8 * b + i) * 1152);
            const f2 dv = unpk(ld32<0>(mkrs(p.dv, D * 2), vo, 0));
            const v4u kvc[2] = {*(LAS v4u*)L(L_ST + cp * 16), *(LAS v4u*)L(L_ST + 5120 + cp * 16)};
            cwait_eq(C_GO, 0xFu, 4u);
            f2 xn[8], pb[8];
#pragma unroll
            for (int t = 0; t < 8; ++t) {
                const f2 st = *(LAS f2*)L(L_STAT + t * 8);
                const f2 x = unpk(qq[t]);
                xn[t] = unpk(pk2((x - st.x) * st.y));
                const u32 c0 = kvc[0][(7 - t) >> 1], c1 = kvc[1][(7 - t) >> 1];
                const f2 kv = (t & 1) ? f2{lo16(c0), lo16(c1)} : f2{hi16(c0), hi16(c1)};
                pb[t] = x + xn[t] + xn[t] * (kv * dv);
            }
            u32 vy[2] = {(u32)((cp >> 2) * 16384 + (cp & 3) * 16), (u32)(((cp + 288) >> 2) * 16384 + (cp & 3) * 16)};
#pragma unroll
            for (int t = 0; t < 8; ++t) asm volatile("" : "+v"(xn[t]), "+v"(pb[t]));
            asm volatile("" : "+v"(vy[0]), "+v"(vy[1]));
            STAMP(11);
            if (isConv) {
                bar();
                if (HELP_DN) {
                    const rsrc_t rd = mkrs(p.wd, F * D * 2), rg = mkrs(p.wg, D * F * 2), ru = mkrs(p.wu, D * F * 2);
                    v4u t[10];
                    {
                        const u32 o = cp < 288 ? pf_off(324 + 4 * xi + (u32)cp / 72, (u32)cp % 72, xq) : OOB;
                        t[0] = ld128<0>(rg, o, 0);
                        t[1] = ld128<0>(ru, o, 0);
                    }
#pragma unroll
                    for (int i = 0; i < 8; ++i) t[2 + i] = ld128<0>(rd, (cp < 288 ? cp * 16 : OOB) + 4608 * i, cj * 36864);
#pragma unroll
                    for (int i = 0; i < 10; ++i) asm volatile("" ::"v"(t[i]));
                    STAMP(9);
                }
            }
            const u32 mask = cwait<8>(C_SEL);
            u32 sr[8];
#pragma unroll
            for (int t = 0; t < 8; ++t) sr[t] = ((mask >> t) & 1u) ? OOB : (8 * b + t) * 1152;
            cwait<8>(C_F2);
            STAMP(3);
            if (isConv) {
                cwait<4>(C_YST, 4);
                if (Y_DELAY) __builtin_amdgcn_s_sleep(Y_DELAY);
            } else {
                wait_word<4>(rw, W_Y + (18 * xq + 17) * 16384 + b * 64);
            }
            STAMP(4);
            if (cp < 288) {
                v4u yv[2];
                poll16<2>(rw, vy, W_Y + b * 64, yv);
                STAMP(5);
                const rsrc_t ro = mkrs(p.out, S * D * 2);
#pragma unroll
                for (int t = 0; t < 8; ++t) {
                    const u32 c0 = yv[0][t >> 1], c1 = yv[1][t >> 1];
                    const f2 y = (t & 1) ? f2{hi16(c0), hi16(c1)} : f2{lo16(c0), lo16(c1)};
                    st32<AUX_OUT>(pk2(pb[t] + xn[t] * y), ro, vo, sr[t]);
                }
                STAMP(6);
            }
        }
    }
    {
        u32 extra = 0;
        if (PROBE_COND) extra = 10 * (ts[PROBE_B] - ts[PROBE_A]);
        if (extra > 30000) extra = 30000;
        while (__builtin_amdgcn_s_memrealtime() - pad0 < PADUS * 100 + extra) __builtin_amdgcn_s_sleep(1);
    }
}

extern "C" void kernel_launch(void* const* d_in, const int* in_sizes, int n_in, void* d_out, int out_size, void* d_ws, size_t ws_size,
                              hipStream_t stream) {
    Params p;
    p.q = (const u16*)d_in[0];
    p.k = (const u16*)d_in[1];
    p.v = (const u16*)d_in[2];
    p.sk = (const u16*)d_in[3];
    p.h = (const u16*)d_in[4];
    p.dv = (const u16*)d_in[5];
    p.wr = (const u16*)d_in[7];
    p.wg = (const u16*)d_in[8];
    p.wu = (const u16*)d_in[9];
    p.wd = (const u16*)d_in[10];
    p.out = (u16*)d_out;
    p.ws = (char*)d_ws + (WS_AT_END ? ((ws_size - WS_BYTES) & ~(size_t)4095) : 0);
    fused_kernel<<<256, NT, 0, stream>>>(p);
}
```

```cpp
#define PADUS 40
#define PROBE_A 0
#define PROBE_B 11
#define PROBE_COND (wave==0)
#include <hip/hip_runtime.h>
#include <stdint.h>

typedef unsigned short u16;
typedef unsigned u32;
typedef unsigned long long u64;
typedef u32 v4u __attribute__((ext_vector_type(4)));
typedef u32 v2u __attribute__((ext_vector_type(2)));
typedef float f4 __attribute__((ext_vector_type(4)));
typedef float f2 __attribute__((ext_vector_type(2)));
typedef __bf16 bf8 __attribute__((ext_vector_type(8)));
typedef __bf16 bf2 __attribute__((ext_vector_type(2)));
#define GAS __attribute__((address_space(1)))
#define LAS __attribute__((address_space(3)))
typedef __amdgpu_buffer_rsrc_t rsrc_t;

#define AUX_IN 0
#define CLR_AUX 16
#define AUX_OUT 16
#define WS_AT_END 1
#define H_DELAY 0
#define Q_DELAY 4
#define U_DELAY 6
#define F_DELAY 20
#define K_DELAY 16
#define Y_DELAY 8
#define YDIRECT 0
#define X_DELAY 2
#define CODE_KB 27
#define HELP_C3 1
#define PF1 2
#define SPRIO 1
#define HELP_GU 1
#define HELP_DN 1

constexpr int S = 2048, D = 576, F = 4608;
constexpr int NT = 640;
constexpr u32 SENT = 0xFFFFFFFFu;
constexpr int SC1 = 16;
constexpr u32 SPIN_CAP = 1u << 16;
constexpr u32 OOB = 0x40000000u;

constexpr int XSB = 9344;
constexpr int W_E = 0, W_F3 = 1024, W_F1 = 4096, W_F2 = W_F1 + 2048, W_KEY = W_F2 + 2048, W_XS = W_KEY + 256 * 64, W_U = W_XS + 8 * XSB,
              W_Y = W_U + 256 * 9216, W_PART = W_Y + 144 * 16384, W_END = W_PART + 72 * 18432, W_DBG = W_END;
constexpr int WS_BYTES = W_END + (1 << 20);
static_assert(W_U % 128 == 0 && XSB % 128 == 0, "align");
constexpr int L_C = 0, L_STAT = L_C + 64, L_SEL = L_STAT + 64, L_WM = L_SEL + 64, L_RED = L_WM + 64, L_ST = 512, L_RA = L_ST + 320 * 32;
constexpr int HC = 4160, HCH = 4 * HC, L_H = L_RA, UCH = 290 * 16, L_U = L_H + 4 * HCH, L_Y = L_U + 4 * UCH;
constexpr int L_R = L_RA, L_HA = L_R + 9 * 8 * 128 * 4, HAS = 144, DNS = 1056, L_DN = L_HA + 1280, L_DT = L_DN + 64 * DNS, L_END = L_DT + 8192;
constexpr int L_LK = L_RA, L_LM = L_LK + 8192 + 128;
static_assert(L_Y + 16384 <= L_END && L_RA % 256 == 0 && L_DN % 256 == 0, "lds");
enum { C_CLR = 0, C_F1 = 1, C_F2 = 2, C_SEL = 3, C_UST = 4, C_CNT = 5, C_ST = 6, C_YST = 7, C_WI = 8, C_GO = 9, C_XS = 10, C_YL = 11, C_TLO = 12, C_THI = 13 };

struct Params {
    const u16 *q, *k, *v, *sk, *h, *dv, *wr, *wg, *wu, *wd;
    u16* out;
    char* ws;
};

__device__ __forceinline__ float asf(u32 x) { return __builtin_bit_cast(float, x); }
__device__ __forceinline__ u32 asu(float x) { return __builtin_bit_cast(u32, x); }
__device__ __forceinline__ float lo16(u32 x) { return asf(x << 16); }
__device__ __forceinline__ float hi16(u32 x) { return asf(x & 0xFFFF0000u); }
__device__ __forceinline__ f2 unpk(u32 x) { return f2{lo16(x), hi16(x)}; }
__device__ __forceinline__ u32 pk(float a, float b) { return __builtin_bit_cast(u32, __builtin_convertvector(f2{a, b}, bf2)); }
__device__ __forceinline__ u32 pk2(f2 a) { return pk(a.x, a.y); }
__device__ __forceinline__ float rbf(float a) { return lo16(pk(a, a)); }
__device__ __forceinline__ u32 nos(u32 x) { return x < 0xFFFFFFFEu ? x : 0xFFFFFFFEu; }
__device__ __forceinline__ u32 umax(u32 a, u32 b) { return a > b ? a : b; }
__device__ __forceinline__ u32 umin(u32 a, u32 b) { return a < b ? a : b; }
__device__ __forceinline__ bool valid4(v4u v) { return umax(umax(v.x, v.y), umax(v.z, v.w)) != SENT; }
__device__ __forceinline__ rsrc_t mkrs(const void* p, u32 bytes) { return __builtin_amdgcn_make_buffer_rsrc((void*)p, 0, bytes, 0x00020000); }
template <int AUX> __device__ __forceinline__ u32 ld32(rsrc_t r, u32 vo, u32 so) { return __builtin_amdgcn_raw_buffer_load_b32(r, vo, so, AUX); }
template <int AUX> __device__ __forceinline__ v2u ld64(rsrc_t r, u32 vo, u32 so) { return __builtin_amdgcn_raw_buffer_load_b64(r, vo, so, AUX); }
template <int AUX> __device__ __forceinline__ v4u ld128(rsrc_t r, u32 vo, u32 so) { return __builtin_amdgcn_raw_buffer_load_b128(r, vo, so, AUX); }
template <int AUX> __device__ __forceinline__ void st32(u32 v, rsrc_t r, u32 vo, u32 so) { __builtin_amdgcn_raw_buffer_store_b32(v, r, vo, so, AUX); }
template <int AUX> __device__ __forceinline__ void st64(v2u v, rsrc_t r, u32 vo, u32 so) { __builtin_amdgcn_raw_buffer_store_b64(v, r, vo, so, AUX); }
template <int AUX> __device__ __forceinline__ void st128(v4u v, rsrc_t r, u32 vo, u32 so) { __builtin_amdgcn_raw_buffer_store_b128(v, r, vo + so, 0, AUX); }
#define FENCE() asm volatile("" ::: "memory")
#define DRAIN() asm volatile("s_waitcnt vmcnt(0)" ::: "memory")
#define LGKM0() asm volatile("s_waitcnt lgkmcnt(0)" ::: "memory")
#define SB() __builtin_amdgcn_sched_barrier(0)
#define NOUNROLL _Pragma("unroll 1")
__device__ __forceinline__ void bar() {
    SB();
    LGKM0();
    __builtin_amdgcn_s_barrier();
    FENCE();
    SB();
}
template <int CTRL> __device__ __forceinline__ u32 dpp(u32 x) { return (u32)__builtin_amdgcn_update_dpp(0, (int)x, CTRL, 0xF, 0xF, true); }
__device__ __forceinline__ float sum32(float x) {
    x += asf(dpp<0xB1>(asu(x)));
    x += asf(dpp<0x4E>(asu(x)));
    x += asf(dpp<0x141>(asu(x)));
    x += asf(dpp<0x140>(asu(x)));
    auto r = __builtin_amdgcn_permlane16_swap(asu(x), asu(x), false, false);
    return asf(r[0]) + asf(r[1]);
}
__device__ __forceinline__ u32 max32(u32 x) {
    x = umax(x, dpp<0xB1>(x));
    x = umax(x, dpp<0x4E>(x));
    x = umax(x, dpp<0x141>(x));
    x = umax(x, dpp<0x140>(x));
    auto r = __builtin_amdgcn_permlane16_swap(x, x, false, false);
    return umax(r[0], r[1]);
}
__device__ __forceinline__ f4 mfma(v4u a, v4u b, f4 c) {
    return __builtin_amdgcn_mfma_f32_16x16x32_bf16(__builtin_bit_cast(bf8, a), __builtin_bit_cast(bf8, b), c, 0, 0, 0);
}

#define STAMP(i) do { if ((i) == PROBE_A || (i) == PROBE_B) { SB(); ts[i] = (u32)__builtin_amdgcn_s_memrealtime(); SB(); } } while (0)

__shared__ __attribute__((aligned(16))) char lds_raw[L_END];

__device__ __forceinline__ LAS char* L(int off) { return (LAS char*)lds_raw + off; }
__device__ __forceinline__ void cset(int w, u32 v = 1u) { *(volatile LAS u32*)L(L_C + 4 * w) = v; }
template <int SL = 1> __device__ __forceinline__ u32 cwait(int w, u32 want = 1u) {
    u32 x = 0;
    NOUNROLL
    for (u32 n = 0; n < (1u << 22); ++n) {
        x = __builtin_amdgcn_readfirstlane(*(volatile LAS u32*)L(L_C + 4 * w));
        if (x >= want) break;
        if (SL) __builtin_amdgcn_s_sleep(SL);
    }
    FENCE();
    return x;
}
__device__ __forceinline__ u32 cpeek(int w) { return *(volatile LAS u32*)L(L_C + 4 * w); }
__device__ __forceinline__ void cadd(int w, u32 v = 1u) {
    FENCE();
    if ((threadIdx.x & 63) == 0) __hip_atomic_fetch_add((LAS u32*)L(L_C + 4 * w), v, __ATOMIC_RELAXED, __HIP_MEMORY_SCOPE_WORKGROUP);
}
template <int SL = 1> __device__ __forceinline__ void cwait_eq(int w, u32 mask, u32 want) {
    NOUNROLL
    for (u32 n = 0; n < (1u << 22); ++n) {
        if ((__builtin_amdgcn_readfirstlane(*(volatile LAS u32*)L(L_C + 4 * w)) & mask) == want) break;
        if (SL) __builtin_amdgcn_s_sleep(SL);
    }
    FENCE();
}
__device__ __forceinline__ void wait_flags(rsrc_t rw, int base, u32 tlo, u32 thi, int lane) {
    NOUNROLL
    for (u32 n = 0; n < SPIN_CAP; ++n) {
        FENCE();
        const v4u f0 = ld128<SC1>(rw, lane * 16, base), f1 = ld128<SC1>(rw, lane * 16 + 1024, base);
        if (__all((((f0.x ^ tlo) | (f0.z ^ tlo) | (f1.x ^ tlo) | (f1.z ^ tlo)) | ((f0.y ^ thi) | (f0.w ^ thi) | (f1.y ^ thi) | (f1.w ^ thi))) == 0u)) break;
        __builtin_amdgcn_s_sleep(8);
    }
    FENCE();
}
__device__ __forceinline__ void wait_flag(rsrc_t rw, int off, u32 tlo, u32 thi) {
    NOUNROLL
    for (u32 n = 0; n < SPIN_CAP; ++n) {
        FENCE();
        const v2u f = ld64<SC1>(rw, 0, off);
        if (f.x == tlo && f.y == thi) break;
        __builtin_amdgcn_s_sleep(4);
    }
    FENCE();
}
template <int SL> __device__ __forceinline__ void wait_word(rsrc_t rw, u32 off) {
    NOUNROLL
    for (u32 n = 0; n < SPIN_CAP; ++n) {
        FENCE();
        if (ld32<SC1>(rw, 0, off) != SENT) break;
        __builtin_amdgcn_s_sleep(SL);
    }
    FENCE();
}
template <int N> __device__ __forceinline__ void poll_issue(rsrc_t rw, const u32 (&vo)[N], u32 so, v4u (&v)[N]) {
    FENCE();
#pragma unroll
    for (int i = 0; i < N; ++i) v[i] = ld128<SC1>(rw, vo[i], so);
    SB();
}
template <int N> __device__ __forceinline__ void poll_finish(rsrc_t rw, const u32 (&vo)[N], u32 so, v4u (&v)[N]) {
    NOUNROLL
    for (u32 n = 0; n < SPIN_CAP; ++n) {
        bool ok[N], all = true;
#pragma unroll
        for (int i = 0; i < N; ++i) { ok[i] = valid4(v[i]); all &= ok[i]; }
        if (__all(all)) break;
        FENCE();
#pragma unroll
        for (int i = 0; i < N; ++i) if (!ok[i]) v[i] = ld128<SC1>(rw, vo[i], so);
    }
    FENCE();
}
template <int N> __device__ __forceinline__ void poll16(rsrc_t rw, const u32 (&vo)[N], u32 so, v4u (&v)[N]) {
    poll_issue<N>(rw, vo, so, v);
    poll_finish<N>(rw, vo, so, v);
}
__device__ __forceinline__ u32 selmask(int b) {
    const int lane = threadIdx.x & 63;
    const u32 rel = (2047u - (*(LAS u32*)L(L_SEL + (lane & 7) * 8) & 0xFFFFu)) - 8u * b;
    u32 m = rel < 8u ? 1u << rel : 0u;
    m |= dpp<0xB1>(m);
    m |= dpp<0x4E>(m);
    m |= dpp<0x141>(m);
    return m;
}

#define DSR64(dst, addr, off) asm volatile("ds_read_b64 %0, %1 offset:%2" : "=v"(dst) : "v"(addr), "i"(off))
#define DSR128(dst, addr, off) asm volatile("ds_read_b128 %0, %1 offset:%2" : "=v"(dst) : "v"(addr), "i"(off))
#define MFMA_IP(acc, a, b) asm volatile("v_mfma_f32_16x16x32_bf16 %0, %1, %2, %0" : "+v"(acc) : "v"(a), "v"(b))

struct AF { v2u lo, hi; };
__device__ __forceinline__ v4u cat(const AF& a) { return v4u{a.lo.x, a.lo.y, a.hi.x, a.hi.y}; }

template <int I0> __device__ __forceinline__ void conv_phase(u32 va, u32 vb, f4 (&acc)[8], AF (&alo)[4], v4u (&blo)[4], AF (&alo2)[4], v4u (&blo2)[4]) {
    AF ahi[4];
    v4u bhi[4];
    constexpr int OA = 64 * I0, OB = 448 - 64 * I0;
    asm volatile("s_waitcnt lgkmcnt(0)" : "+v"(alo[0].lo), "+v"(alo[0].hi), "+v"(alo[1].lo), "+v"(alo[1].hi), "+v"(alo[2].lo), "+v"(alo[2].hi), "+v"(alo[3].lo),
                 "+v"(alo[3].hi), "+v"(blo[0]), "+v"(blo[1]), "+v"(blo[2]), "+v"(blo[3]));
    SB();
#pragma unroll
    for (int n = 0; n < 16; ++n) {
        const int e = n >> 2, b = n & 3;
        MFMA_IP(acc[e + b], cat(alo[e]), blo[b]);
        if (n < 4) DSR128(bhi[n], vb, OB + 512 * (4 + n));
        else if (n < 12) {
            if (n & 1) DSR64(ahi[(n - 4) >> 1].hi, va, OA + 512 * (4 + ((n - 4) >> 1)) + 8);
            else DSR64(ahi[(n - 4) >> 1].lo, va, OA + 512 * (4 + ((n - 4) >> 1)));
        }
        SB();
    }
    asm volatile("s_waitcnt lgkmcnt(0)" : "+v"(ahi[0].lo), "+v"(ahi[0].hi), "+v"(ahi[1].lo), "+v"(ahi[1].hi), "+v"(ahi[2].lo), "+v"(ahi[2].hi), "+v"(ahi[3].lo),
                 "+v"(ahi[3].hi), "+v"(bhi[0]), "+v"(bhi[1]), "+v"(bhi[2]), "+v"(bhi[3]));
    SB();
    int n = 0;
#pragma unroll
    for (int half = 0; half < 2; ++half)
#pragma unroll
        for (int e = 0; e < 4; ++e)
#pragma unroll
            for (int b = 0; b < 4 - e; ++b) {
                if (half == 0) MFMA_IP(acc[4 + e + b], cat(alo[e]), bhi[b]);
                else MFMA_IP(acc[4 + e + b], cat(ahi[e]), blo[b]);
                if (I0 < 7) {
                    if (n < 4) DSR128(blo2[n], vb, OB - 64 + 512 * n);
                    else if (n < 12) {
                        if (n & 1) DSR64(alo2[(n - 4) >> 1].hi, va, OA + 64 + 512 * ((n - 4) >> 1) + 8);
                        else DSR64(alo2[(n - 4) >> 1].lo, va, OA + 64 + 512 * ((n - 4) >> 1));
                    }
                }
                ++n;
                SB();
            }
}

__device__ __forceinline__ u32 pf_off(u32 idx, u32 ch, int xq) {
    const u32 g = idx / 36u, rest = idx - 36u * g;
    const u32 row = 64u * (rest >> 2) + 8u * (rest & 3u) + (g < 3u ? 5u + g : 29u + g);
    return row * (F * 2) + xq * 1152 + ch * 16;
}

template <class TS>
__device__ __forceinline__ void ln_part(rsrc_t rw, int b, int lw, int lane, const u32 (&xq9)[11], const u32 (&wr9)[9], TS& ts) {
    const int hf = lane >> 5, l32 = lane & 31, tl = 2 * lw + hf, tok = 8 * b + tl;
    u32 ko = l32 == 0 ? W_KEY + b * 64 + tl * 8 : OOB;
    asm volatile("" : "+v"(ko));
    asm volatile("" ::"v"(xq9[8]));
    STAMP(10);
    f2 x[9];
    f2 s2 = {0.f, 0.f};
#pragma unroll
    for (int i = 0; i < 9; ++i) { x[i] = unpk(xq9[i]); s2 += x[i]; }
    const float mean = sum32(s2.x + s2.y) * (1.0f / D);
    f2 v2 = {0.f, 0.f};
#pragma unroll
    for (int i = 0; i < 9; ++i) { const f2 dl = x[i] - mean; v2 += dl * dl; }
    const u32 clr = cpeek(C_CLR);
    const float var = rbf(sum32(v2.x + v2.y) * (1.0f / D));
    const float mb = rbf(mean), sd = rbf(__builtin_amdgcn_sqrtf(rbf(var + 1e-5f)));
    const float inv = __builtin_amdgcn_rcpf(sd);
    float a0 = 0.f, a1 = 0.f;
#pragma unroll
    for (int i = 0; i < 9; ++i) {
        const u32 xn = pk2((x[i] - mb) * inv);
        if (i & 1) a1 = __builtin_amdgcn_fdot2_f32_bf16(__builtin_bit_cast(bf2, xn), __builtin_bit_cast(bf2, wr9[i]), a1, false);
        else a0 = __builtin_amdgcn_fdot2_f32_bf16(__builtin_bit_cast(bf2, xn), __builtin_bit_cast(bf2, wr9[i]), a0, false);
    }
    const float rs = sum32(a0 + a1);
    const u32 rb = pk(rs, rs) & 0xFFFFu;
    u32 ord = (rb & 0x8000u) ? (~rb & 0xFFFFu) : (rb | 0x8000u);
    if ((rb & 0x7FFFu) > 0x7F80u) ord = 0u;
    v2u rec = {(ord << 16) | (u32)(2047 - tok), nos(pk(mb, sd))};
    asm volatile("" : "+v"(rec));
    STAMP(1);
    if (!__builtin_amdgcn_readfirstlane(clr)) cwait(C_CLR);
    st64<SC1>(rec, rw, ko, 0);
    if (l32 == 0) *(LAS f2*)L(L_STAT + tl * 8) = f2{mb, inv};
    cadd(C_GO);
    STAMP(2);
}

typedef u32 v16u __attribute__((ext_vector_type(16)));
typedef u32 v8u __attribute__((ext_vector_type(8)));
__device__ __forceinline__ u64 mk64(u32 lo, u32 hi) { return ((u64)hi << 32) | lo; }

__global__ void __launch_bounds__(NT, 3) fused_kernel(Params) {
    v16u ka;
    v8u kb;
    asm volatile("s_load_dwordx16 %0, %2, 0x0\n\ts_load_dwordx8 %1, %2, 0x40" : "=&s"(ka), "=&s"(kb) : "s"(__builtin_amdgcn_kernarg_segment_ptr()));
    const int tid = threadIdx.x, lane = tid & 63, wave = __builtin_amdgcn_readfirstlane(tid >> 6);
    const int wg = blockIdx.x, xq = wg & 7, xi = wg >> 3, b = 32 * xq + xi;
    const bool isConv = xi < 18, isMoe = xi >= 18 && xi < 27, isSel = xi == 27;
    const int cj = 18 * xq + xi, mu = 9 * xq + xi - 18;
    const u32 xs = W_XS + xq * XSB;
    u32 ts[12];
#pragma unroll
    for (int i = 0; i < 12; ++i) ts[i] = 0;
    u64 pc;
    asm volatile("s_getpc_b64 %0" : "=s"(pc));
    STAMP(0);
    const u64 pad0 = __builtin_amdgcn_s_memrealtime();
    if (SPRIO && wave == 0) __builtin_amdgcn_s_setprio(3);
    const bool isK = wave >= 1 && wave < 6, isL = wave >= 6;
    const int cp = tid - 64;
    const int lw = wave - 6;
    const int mw = wave - 1;
    u32 so[12], nsk = isK ? D * 8 : 0, nv = isK ? S * D * 2 : (isL ? 384 + D * 2 : 0);
    {
        u32 s0 = isL ? 0u : (u32)(8 * b - 3) * 1152u, st = isL ? 128u : 1152u;
        asm volatile("" : "+s"(s0), "+s"(st), "+s"(nsk), "+s"(nv));
#pragma unroll
        for (int i = 0; i < 12; ++i) {
            so[i] = s0 + i * st;
            asm volatile("" : "+s"(so[i]));
        }
    }
    u32 va0 = (u32)cp < 288u ? cp * 4 : OOB, va1 = (8 * b + 2 * lw + (lane >> 5)) * 1152 + (lane & 31) * 4;
    asm volatile("" : "+v"(va0), "+v"(va1));
    u32 vb0 = tid >= 384 ? (lane & 31) * 4 : va0, vb1 = tid >= 384 ? (lane & 31) * 4 : OOB;
    va0 = tid >= 384 ? va1 : va0;
    asm volatile("" : "+v"(va0), "+v"(vb0), "+v"(vb1));
    SB();
    v4u pf = {0u, 0u, 0u, 0u};
    if (isL) {
        if (CODE_KB && xi * 4 + lw < CODE_KB) pf = *(const GAS v4u*)((pc & ~255ull) + (xi * 4 + lw) * 1024 + lane * 16);
        if (Q_DELAY) __builtin_amdgcn_s_sleep(Q_DELAY);
    }
    SB();
    asm volatile("s_waitcnt lgkmcnt(0)" : "+s"(ka), "+s"(kb));
    Params p;
    p.q = (const u16*)mk64(ka[0], ka[1]);
    p.k = (const u16*)mk64(ka[2], ka[3]);
    p.v = (const u16*)mk64(ka[4], ka[5]);
    p.sk = (const u16*)mk64(ka[6], ka[7]);
    p.h = (const u16*)mk64(ka[8], ka[9]);
    p.dv = (const u16*)mk64(ka[10], ka[11]);
    p.wr = (const u16*)mk64(ka[12], ka[13]);
    p.wg = (const u16*)mk64(ka[14], ka[15]);
    p.wu = (const u16*)mk64(kb[0], kb[1]);
    p.wd = (const u16*)mk64(kb[2], kb[3]);
    p.out = (u16*)mk64(kb[4], kb[5]);
    p.ws = (char*)mk64(kb[6], kb[7]);
    const rsrc_t rw = mkrs(p.ws, WS_BYTES);
    const rsrc_t rq = mkrs(p.q, S * D * 2);

    u64 pa = isL ? (u64)p.q : (u64)p.k, pb = isL ? (u64)p.wr - 384 : (u64)p.v;
    asm volatile("" : "+s"(pa), "+s"(pb));
    const v4u s4 = {SENT, SENT, SENT, SENT};
    u32 ra[11], rb[9];
    v4u sk;
    if (wave != 0) {
        sk = ld128<0>(mkrs(p.sk, nsk), cp * 16, 0);
        const rsrc_t da = mkrs((const void*)pa, S * D * 2), db = mkrs((const void*)pb, nv);
#pragma unroll
        for (int i = 0; i < 11; ++i) {
            ra[i] = ld32<AUX_IN>(da, va0, so[i]);
            if (i >= 3) rb[i - 3] = ld32<AUX_IN>(db, vb0, so[i]);
        }
        rb[8] = ld32<AUX_IN>(db, vb1, so[11]);
    }
    SB();
    v2u e = {0u, 0u};
    if (wave == 0) {
#pragma unroll
        for (int i = 0; i < 9; ++i) st128<SC1>(s4, rw, lane * 16 + 1024 * i, W_U + b * 9216);
        st128<SC1>(s4, rw, lane < 4 ? W_KEY + b * 64 + lane * 16 : OOB, 0);
        e = ld64<SC1>(rw, 0, W_E);
        if (lane < 16) *(LAS u32*)L(L_C + 4 * lane) = 0u;
    }
    STAMP(11);
    bar();
    STAMP(8);

    if (wave == 0) {
        if (SPRIO) __builtin_amdgcn_s_setprio(0);
        DRAIN();
        const u64 tag = (((u64)e.y << 32) | e.x) + 1ull;
        const u32 tlo = __builtin_amdgcn_readfirstlane((u32)tag), thi = __builtin_amdgcn_readfirstlane((u32)(tag >> 32));
        if (lane == 0) st64<SC1>(v2u{tlo, thi}, rw, 0, W_F1 + wg * 8);
        cset(C_TLO, tlo);
        cset(C_THI, thi);
        cset(C_CLR);
        STAMP(1);
        if (isMoe) {
#pragma unroll
            for (int i = 0; i < 18; ++i) st128<SC1>(s4, rw, lane * 16 + 1024 * i, W_PART + mu * 18432);
            cadd(C_GO, 0x100u);
            DRAIN();
            if (lane == 0) st64<SC1>(v2u{tlo, thi}, rw, 0, W_F2 + wg * 8);
            STAMP(2);
            bar();
            bar();
            {
                const v4u sd = {(u32)(u64)p.wd, (u32)((u64)p.wd >> 32) & 0xFFFFu, (u32)(F * D * 2), 0x00020000u};
                u32 ro = 64 * mu * 1152;
                asm volatile("s_mov_b32 m0, %2\n\ts_nop 0\n\t.rept 64\n\tbuffer_load_dwordx4 %1, %3, %0 offen lds\n\ts_add_u32 m0, m0, 1056\n\t"
                             "s_add_u32 %0, %0, 1152\n\t.endr"
                             : "+s"(ro) : "v"(lane * 16), "s"((u32)(uintptr_t)L(L_DN)), "s"(sd) : "memory", "scc");
                ro = 64 * mu * 1152;
                asm volatile("s_mov_b32 m0, %2\n\ts_nop 0\n\t.rept 8\n\tbuffer_load_dwordx4 %1, %3, %0 offen lds\n\ts_add_u32 m0, m0, 1024\n\t"
                             "s_add_u32 %0, %0, 9216\n\t.endr"
                             : "+s"(ro) : "v"((lane >> 3) * 1152 + 1024 + (lane & 7) * 16), "s"((u32)(uintptr_t)L(L_DT)), "s"(sd) : "memory", "scc");
                STAMP(3);
                DRAIN();
                STAMP(4);
            }
            bar();
            bar();
            u32 vo[1] = {(u32)(lane & 3) * 16};
            v4u sv[1];
            poll16<1>(rw, vo, xs, sv);
            if (lane < 4) *(LAS v4u*)L(L_SEL + lane * 16) = sv[0];
            LGKM0();
            cset(C_SEL, 0x100u | selmask(b));
            wait_flags(rw, W_F2, tlo, thi, lane);
            cset(C_F2);
        } else if (isSel) {
#pragma unroll
            for (int i = 0; i < 9; ++i) st128<SC1>(s4, rw, lane * 16 + 1024 * i, xs + 128);
            st128<SC1>(s4, rw, lane < 4 ? xs + lane * 16 : OOB, 0);
            DRAIN();
            if (lane == 0) {
                st64<SC1>(v2u{tlo, thi}, rw, 0, W_F2 + wg * 8);
                st64<SC1>(v2u{tlo, thi}, rw, 0, W_F3 + xq * 128);
            }
            cwait_eq(C_GO, 0xFu, 4u);
            if (K_DELAY) __builtin_amdgcn_s_sleep(K_DELAY);
            wait_flags(rw, W_F1, tlo, thi, lane);
            STAMP(2);
            bar();
            if (xq == 0 && lane == 0) st64<SC1>(v2u{tlo, thi}, rw, 0, W_E);
            bar();
            bar();
            bar();
            cset(C_SEL, 0x100u | selmask(b));
            __builtin_amdgcn_s_sleep(32);
            wait_flags(rw, W_F2, tlo, thi, lane);
            cset(C_F2);
        } else {
            if (isConv) {
                if (F_DELAY) __builtin_amdgcn_s_sleep(F_DELAY);
                wait_flags(rw, W_F1, tlo, thi, lane);
                cset(C_F1);
                STAMP(2);
#pragma unroll
                for (int i = 0; i < 16; ++i) st128<SC1>(s4, rw, lane * 16 + 1024 * i, W_Y + cj * 16384);
                bar();
                DRAIN();
                cadd(C_YL);
                if (lane == 0) st64<SC1>(v2u{tlo, thi}, rw, 0, W_F2 + wg * 8);
            } else {
                if (lane == 0) st64<SC1>(v2u{tlo, thi}, rw, 0, W_F2 + wg * 8);
                cwait<4>(C_GO, 0x54u);
                __builtin_amdgcn_s_sleep(32);
            }
            wait_flag(rw, W_F3 + xq * 128, tlo, thi);
            STAMP(3);
            u32 vo[1] = {(u32)(lane & 3) * 16};
            v4u sv[1];
            wait_word<16>(rw, xs + 60);
            poll16<1>(rw, vo, xs, sv);
            if (lane < 4) *(LAS v4u*)L(L_SEL + lane * 16) = sv[0];
            LGKM0();
            cset(C_SEL, 0x100u | selmask(b));
            STAMP(4);
            wait_flags(rw, W_F2, tlo, thi, lane);
            cset(C_F2);
            STAMP(5);
            if (isConv) {
                const int s = cj / 18, cb = cj % 18, d = 32 * cb + (lane & 31);
                const v2u me = *(LAS v2u*)L(L_SEL + s * 8);
                const int tok = (2047 - me.x) & 2047;
                const u32 ord = me.x >> 16, rb = (ord & 0x8000u) ? (ord & 0x7FFFu) : (~ord & 0xFFFFu);
                const float mb = lo16(me.y), inv = __builtin_amdgcn_rcpf(hi16(me.y));
                const u32 eo = (tok * D + d) * 2;
                const rsrc_t rk = mkrs(p.k, S * D * 2);
                const u32 xb = __builtin_amdgcn_raw_buffer_load_b16(rq, eo, 0, 0), vb = __builtin_amdgcn_raw_buffer_load_b16(mkrs(p.v, S * D * 2), eo, 0, 0);
                u32 kb[4];
#pragma unroll
                for (int j = 0; j < 4; ++j) kb[j] = __builtin_amdgcn_raw_buffer_load_b16(rk, tok >= j ? eo - j * 1152 : OOB, 0, 0);
                const v2u sw = ld64<0>(mkrs(p.sk, D * 8), d * 8, 0);
                const u32 db = __builtin_amdgcn_raw_buffer_load_b16(mkrs(p.dv, D * 2), d * 2, 0, 0);
                const float x = lo16(xb);
                const float kv = rbf((lo16(kb[3]) * hi16(sw.y) + lo16(kb[2]) * lo16(sw.y) + lo16(kb[1]) * hi16(sw.x) + lo16(kb[0]) * lo16(sw.x)) * lo16(vb));
                const float xn = rbf((x - mb) * inv);
                float base = x + xn * kv * lo16(db);
                float rsg = rbf(__builtin_amdgcn_rcpf(1.0f + __expf(-lo16(rb))));
                const int c = (d & 1) * 288 + (d >> 1);
                u32 yo = (c >> 2) * 16384 + (c & 3) * 16 + (tok >> 3) * 64 + ((tok & 7) >> 1) * 4;
                u32 vp[9];
#pragma unroll
                for (int i = 0; i < 9; ++i) vp[i] = (8 * i + (lane >> 3)) * 18432 + (lane & 7) * 16;
                asm volatile("" : "+v"(base), "+v"(rsg), "+v"(yo));
                wait_word<4>(rw, W_PART + (8 * (cj % 9) + 3) * 18432 + s * 2304 + cb * 128);
                cwait<2>(C_YST, 4);
                if (Y_DELAY) __builtin_amdgcn_s_sleep(Y_DELAY);
                STAMP(6);
                v4u pv[9];
                u32 yw = ld32<SC1>(rw, yo, W_Y);
                poll16<9>(rw, vp, W_PART + s * 2304 + cb * 128, pv);
                STAMP(7);
                f4 a = {0.f, 0.f, 0.f, 0.f};
#pragma unroll
                for (int i = 0; i < 9; ++i) a += __builtin_bit_cast(f4, pv[i]);
#pragma unroll
                for (int i = 0; i < 4; ++i) {
                    float t = a[i];
                    t += asf(dpp<0x128>(asu(t)));
                    const auto r1 = __builtin_amdgcn_permlane16_swap(asu(t), asu(t), false, false);
                    t = asf(r1[0]) + asf(r1[1]);
                    const auto r2 = __builtin_amdgcn_permlane32_swap(asu(t), asu(t), false, false);
                    a[i] = asf(r2[0]) + asf(r2[1]);
                }
                if (lane < 8) *(LAS f4*)L(L_RED + lane * 16) = a;
                LGKM0();
                base += rbf(xn + rbf(rbf(*(LAS float*)L(L_RED + (lane & 31) * 4)) * rsg));
                asm volatile("" : "+v"(base));
                STAMP(8);
                NOUNROLL
                for (u32 n = 0; n < SPIN_CAP; ++n) {
                    if (__all(yw != SENT)) break;
                    FENCE();
                    yw = ld32<SC1>(rw, yo, W_Y);
                }
                const float o = base + xn * ((tok & 1) ? hi16(yw) : lo16(yw));
                if (lane < 32) __builtin_amdgcn_raw_buffer_store_b16((u16)pk(o, 0.f), mkrs(p.out, S * D * 2), eo, 0, AUX_OUT);
                STAMP(9);
            }
        }
    } else {
        if (isK) {
            u32 uo = cp < 288 ? W_U + b * 9216 + cp * 16 : OOB;
            u32 pfo[2];
#pragma unroll
            for (int i = 0; i < 2; ++i) pfo[i] = cp < 288 ? pf_off(4 * (18 * i + xi) + (u32)cp / 72, (u32)cp % 72, xq) : OOB;
            asm volatile("" : "+v"(uo), "+v"(pfo[0]), "+v"(pfo[1]));
            const f2 w0 = {lo16(sk.x), lo16(sk.z)}, w1 = {hi16(sk.x), hi16(sk.z)}, w2 = {lo16(sk.y), lo16(sk.w)}, w3 = {hi16(sk.y), hi16(sk.w)};
            f2 kf[11], r[8];
            asm volatile("" ::"v"(rb[7]));
            STAMP(10);
            SB();
            v4u hd[4];
            {
                const int c = 4 * cj + mw;
                const rsrc_t rh = mkrs(p.h, isConv && mw < 4 ? D * S * 2 : 0);
#pragma unroll
                for (int i = 0; i < 4; ++i) hd[i] = ld128<AUX_IN>(rh, lane * 16 + 1024 * i, (c < 288 ? 2 * c : 2 * (c - 288) + 1) * 4096);
            }
            v4u t1[4] = {s4, s4, s4, s4};
            if (PF1 == 1) {
                const rsrc_t rg = mkrs(p.wg, isConv ? D * F * 2 : 0), ru = mkrs(p.wu, isConv ? D * F * 2 : 0);
#pragma unroll
                for (int i = 0; i < 2; ++i) {
                    t1[2 * i] = ld128<0>(rg, pfo[i], 0);
                    t1[2 * i + 1] = ld128<0>(ru, pfo[i], 0);
                }
            }
            SB();
#pragma unroll
            for (int i = 0; i < 11; ++i) kf[i] = unpk(ra[i]);
#pragma unroll
            for (int t = 0; t < 8; ++t) r[t] = (kf[t] * w3 + kf[t + 1] * w2 + kf[t + 2] * w1 + kf[t + 3] * w0) * unpk(rb[t]);
            const u32 clr = cpeek(C_CLR);
            v4u c0 = v4u{nos(pk(r[7].x, r[6].x)), nos(pk(r[5].x, r[4].x)), nos(pk(r[3].x, r[2].x)), nos(pk(r[1].x, r[0].x))};
            v4u c1 = v4u{nos(pk(r[7].y, r[6].y)), nos(pk(r[5].y, r[4].y)), nos(pk(r[3].y, r[2].y)), nos(pk(r[1].y, r[0].y))};
            asm volatile("" : "+v"(c0), "+v"(c1));
            STAMP(1);
            if (!__builtin_amdgcn_readfirstlane(clr)) cwait(C_CLR);
            st128<SC1>(c0, rw, uo, 0);
            st128<SC1>(c1, rw, uo + 4608, 0);
            cadd(C_GO, 0x10u);
            STAMP(2);
            if (PF1 == 2) {
                const rsrc_t rg = mkrs(p.wg, isConv ? D * F * 2 : 0), ru = mkrs(p.wu, isConv ? D * F * 2 : 0);
#pragma unroll
                for (int i = 0; i < 2; ++i) {
                    t1[2 * i] = ld128<0>(rg, pfo[i], 0);
                    t1[2 * i + 1] = ld128<0>(ru, pfo[i], 0);
                }
            }
            *(LAS v4u*)L(L_ST + cp * 16) = c0;
            *(LAS v4u*)L(L_ST + 5120 + cp * 16) = c1;
            if (isConv && mw < 4) {
                LAS char* hb = L(L_H + mw * HCH);
#pragma unroll
            for (int i = 0; i < 4; ++i) {
                const v4u dd = hd[i];
                const u32 e0 = i < 3 ? __builtin_amdgcn_readfirstlane(hd[i < 3 ? i + 1 : 3].x) : 0u;
                const u32 e1 = i < 3 ? __builtin_amdgcn_readfirstlane(hd[i < 3 ? i + 1 : 3].y) : 0u;
                const u32 n0 = __builtin_amdgcn_update_dpp(e0, dd.x, 0x130, 0xF, 0xF, false);
                const u32 n1 = __builtin_amdgcn_update_dpp(e1, dd.y, 0x130, 0xF, 0xF, false);
                const u32 a01 = __builtin_amdgcn_alignbit(dd.y, dd.x, 16), a12 = __builtin_amdgcn_alignbit(dd.z, dd.y, 16),
                          a23 = __builtin_amdgcn_alignbit(dd.w, dd.z, 16), a3n = __builtin_amdgcn_alignbit(n0, dd.w, 16),
                          ann = __builtin_amdgcn_alignbit(n1, n0, 16);
                LAS char* o = hb + (lane + 64 * i + 2) * 16;
                *(LAS v4u*)(o) = dd;
                *(LAS v4u*)(o + HC) = v4u{a01, a12, a23, a3n};
                *(LAS v4u*)(o + 2 * HC) = v4u{dd.y, dd.z, dd.w, n0};
                *(LAS v4u*)(o + 3 * HC) = v4u{a12, a23, a3n, ann};
            }
            {
                const u32 h0 = __builtin_amdgcn_readfirstlane(hd[0].x), h1 = __builtin_amdgcn_readfirstlane(hd[0].y);
                if (lane < 8) {
                    const bool od = lane & 1;
                    const int sg = lane >> 1;
                    v4u z = {0u, 0u, 0u, 0u};
                    z.z = od && sg == 3 ? h0 << 16 : 0u;
                    z.w = !od || sg == 0 ? 0u : (sg == 1 ? h0 << 16 : (sg == 2 ? h0 : (h0 >> 16) | (h1 << 16)));
                    *(LAS v4u*)(hb + sg * HC + (lane & 1) * 16) = z;
                }
            }
                STAMP(7);
            }
            asm volatile("" ::"v"(t1[0]), "v"(t1[1]), "v"(t1[2]), "v"(t1[3]));
            if (isSel) {
                *(LAS v4u*)L(L_LK + cp * 16) = v4u{0u, 0u, 0u, 0u};
                if (cp < 196) *(LAS v4u*)L(L_LK + 5120 + cp * 16) = v4u{0u, 0u, 0u, 0u};
            }
        } else if (isConv) {
            LAS char* hb = L(L_H + lw * HCH);
            const int l = tid - 384;
            if (lane < 32) *(LAS v4u*)L(L_U + lw * UCH + lane * 16) = v4u{0u, 0u, 0u, 0u};
            f4 acc[8];
#pragma unroll
            for (int i = 0; i < 8; ++i) {
                acc[i] = f4{0.f, 0.f, 0.f, 0.f};
                asm volatile("" : "+v"(acc[i]));
            }
            u32 vo[4];
#pragma unroll
            for (int i = 0; i < 4; ++i) vo[i] = (64 * i + (l >> 2)) * 9216 + (l & 3) * 16;
            const int m1 = (lane & 15) + 1, kq = lane >> 4;
            u32 va = (u32)(uintptr_t)(hb + (m1 & 3) * HC + 16 * kq + 8 * (m1 >> 2));
            u32 vb = (u32)(uintptr_t)L(L_U + lw * UCH + (33 + 2 * (lane & 15) - kq) * 16 - 448);
            u32 uw = (u32)(uintptr_t)L(L_U + (l & 3) * UCH + (32 + (l >> 2)) * 16);
            u32 yw = (u32)(uintptr_t)L(L_Y + (32 * (kq & 1) + 2 * (lane & 15) + (kq >> 1)) * 64 + ((lw ^ lane) & 3) * 16);
            u32 yr = (u32)(uintptr_t)L(L_Y + (l >> 2) * 64 + ((l ^ (l >> 3)) & 3) * 16);
            u32 yg = W_Y + cj * 16384 + l * 16;
            asm volatile("" : "+v"(vo[0]), "+v"(vo[1]), "+v"(vo[2]), "+v"(vo[3]), "+v"(va), "+v"(vb), "+v"(uw), "+v"(yw), "+v"(yr), "+v"(yg));
            SB();
            STAMP(3);
            ln_part(rw, b, lw, lane, ra, rb, ts);
            asm volatile("" ::"v"(pf));
            cwait_eq(C_GO, 0xF0u, 0x50u);
            if (U_DELAY) __builtin_amdgcn_s_sleep(U_DELAY);
            cwait(C_F1);
            STAMP(4);
            v4u uv[4];
            poll_issue<4>(rw, vo, W_U + cj * 64, uv);
            poll_finish<4>(rw, vo, W_U + cj * 64, uv);
#pragma unroll
            for (int i = 0; i < 4; ++i) *(LAS v4u*)((LAS char*)(uintptr_t)uw + 1024 * i) = uv[i];
            STAMP(5);
            bar();
            STAMP(8);
            AF a0[4], a1[4];
            v4u b0[4], b1[4];
#pragma unroll
            for (int n = 0; n < 4; ++n) {
                DSR128(b0[n], vb, 448 + 512 * n);
                DSR64(a0[n].lo, va, 512 * n);
                DSR64(a0[n].hi, va, 512 * n + 8);
            }
            conv_phase<0>(va, vb, acc, a0, b0, a1, b1);
            conv_phase<1>(va, vb, acc, a1, b1, a0, b0);
            conv_phase<2>(va, vb, acc, a0, b0, a1, b1);
            conv_phase<3>(va, vb, acc, a1, b1, a0, b0);
            conv_phase<4>(va, vb, acc, a0, b0, a1, b1);
            conv_phase<5>(va, vb, acc, a1, b1, a0, b0);
            conv_phase<6>(va, vb, acc, a0, b0, a1, b1);
            conv_phase<7>(va, vb, acc, a1, b1, a0, b0);
            asm volatile("s_nop 7\n\ts_nop 1" : "+v"(acc[0]), "+v"(acc[1]), "+v"(acc[2]), "+v"(acc[3]), "+v"(acc[4]), "+v"(acc[5]), "+v"(acc[6]), "+v"(acc[7]));
            STAMP(6);
#pragma unroll
            for (int q = 0; q < 8; q += 2) {
                const u32 y0 = nos(pk(acc[q][0], acc[q][1])), y1 = nos(pk(acc[q][2], acc[q][3]));
                const u32 z0 = nos(pk(acc[q + 1][0], acc[q + 1][1])), z1 = nos(pk(acc[q + 1][2], acc[q + 1][3]));
                const auto s0 = __builtin_amdgcn_permlane16_swap(y0, z0, false, false);
                const auto s1 = __builtin_amdgcn_permlane16_swap(y1, z1, false, false);
                *(LAS v4u*)(uintptr_t)(yw + q * 2048) = v4u{s0[0], s1[0], s0[1], s1[1]};
            }
            cadd(C_YL);
            cwait<0>(C_YL, 5);
            {
                v4u yv[4];
#pragma unroll
                for (int i = 0; i < 4; ++i) yv[i] = *(LAS v4u*)(uintptr_t)(yr + 4096 * i);
#pragma unroll
                for (int i = 0; i < 4; ++i) st128<SC1>(yv[i], rw, yg + 4096 * i, 0);
            }
            cadd(C_YST);
            STAMP(7);
        } else {
            ln_part(rw, b, lw, lane, ra, rb, ts);
            asm volatile("" ::"v"(pf));
        }

        if (isMoe) {
            const int c = lane & 15, kq = lane >> 4;
            const GAS char* gp = (const GAS char*)(c < 8 ? p.wg : p.wu) + (size_t)(64 * mw + 8 * kq) * (F * 2) + mu * 128 + (c & 7) * 16;
            v4u gr[2][8];
            cwait(C_GO, 0x154u);
#pragma unroll
            for (int e = 0; e < 8; ++e) gr[0][e] = *(const GAS v4u*)(gp + (size_t)e * (F * 2));
            bar();
#pragma unroll
            for (int e = 0; e < 8; ++e) gr[1][e] = *(const GAS v4u*)(gp + (size_t)(32 + e) * (F * 2));
            bar();
            STAMP(11);
            v4u gf[2][8];
#pragma unroll
            for (int ks = 0; ks < 2; ++ks) {
#pragma unroll
                for (int nn = 0; nn < 8; ++nn)
#pragma unroll
                    for (int pp = 0; pp < 4; ++pp)
                        gf[ks][nn][pp] = __builtin_amdgcn_perm(gr[ks][2 * pp + 1][nn >> 1], gr[ks][2 * pp][nn >> 1], (nn & 1) ? 0x07060302u : 0x05040100u);
#pragma unroll
                for (int nn = 0; nn < 8; ++nn) asm volatile("" : "+v"(gf[ks][nn]));
                SB();
            }
            STAMP(7);
            u32 xo[2] = {128 + (c & 7) * 1152 + (64 * mw + 8 * kq) * 2u, 192 + (c & 7) * 1152 + (64 * mw + 8 * kq) * 2u};
            u32 ro = (u32)(uintptr_t)L(L_R + ((mw * 8 + 4 * (kq & 1)) * 128 + 8 * c) * 4);
            u32 po = W_PART + mu * 18432 + mw * 256 + (c & 7) * 2304 + (c >> 3) * 64 + kq * 16;
            u32 hwo = (u32)(uintptr_t)L(L_HA + mw * HAS + ((lane & 32) + 8 * ((lane & 15) >> 2) + 4 * ((lane >> 4) & 1) + (lane & 3)) * 2);
            asm volatile("" : "+v"(hwo));
            u32 ta[4];
#pragma unroll
            for (int i = 0; i < 4; ++i) {
                const int row = 16 * i + 4 * kq + (c >> 2);
                ta[i] = (u32)(uintptr_t)L(mw < 8 ? L_DN + row * DNS + 128 * mw + 8 * (c & 3) : L_DT + row * 128 + 8 * (c & 3));
                asm volatile("" : "+v"(ta[i]));
            }
            asm volatile("" : "+v"(xo[0]), "+v"(xo[1]), "+v"(ro), "+v"(po));
            STAMP(8);
            if (wave == 9) {
                wait_flag(rw, W_F3 + xq * 128, *(volatile LAS u32*)L(L_C + 4 * C_TLO), *(volatile LAS u32*)L(L_C + 4 * C_THI));
                wait_word<2>(rw, xs + 60);
                cset(C_XS);
            }
            cwait<2>(C_XS);
            if (X_DELAY) __builtin_amdgcn_s_sleep(X_DELAY);
            {
                v4u xa[2];
                STAMP(4);
                poll16<2>(rw, xo, xs, xa);
                STAMP(5);
                const v4u xa0 = xa[0], xa1 = xa[1];
                f4 g[8];
#pragma unroll
                for (int nn = 0; nn < 8; ++nn) g[nn] = mfma(xa1, gf[1][nn], mfma(xa0, gf[0][nn], f4{0.f, 0.f, 0.f, 0.f}));
#pragma unroll
                for (int r = 0; r < 4; ++r) {
                    const f4 o = kq < 2 ? f4{g[0][r], g[1][r], g[2][r], g[3][r]} : f4{g[4][r], g[5][r], g[6][r], g[7][r]};
                    *(LAS f4*)(uintptr_t)(ro + r * 512 + (kq >> 1) * 16) = o;
                }
            }
            bar();
            v2u dl[2][4], dh[2][4];
            {
                float gv[9], uv[9];
#pragma unroll
                for (int w = 0; w < 9; ++w) {
                    gv[w] = *(LAS float*)L(L_R + ((w * 8 + mw) * 128 + lane) * 4);
                    uv[w] = *(LAS float*)L(L_R + ((w * 8 + mw) * 128 + 64 + lane) * 4);
                }
                SB();
                float g = ((gv[0] + gv[1]) + (gv[2] + gv[3])) + ((gv[4] + gv[5]) + (gv[6] + gv[7])) + gv[8];
                float u = ((uv[0] + uv[1]) + (uv[2] + uv[3])) + ((uv[4] + uv[5]) + (uv[6] + uv[7])) + uv[8];
                asm volatile("" : "+v"(g), "+v"(u));
                SB();
#pragma unroll
                for (int ks = 0; ks < 2; ++ks)
#pragma unroll
                    for (int nt = 0; nt < 4; ++nt) {
                        asm volatile("ds_read_b64_tr_b16 %0, %1 offset:%2" : "=v"(dl[ks][nt]) : "v"(ta[2 * ks]), "i"(32 * nt));
                        asm volatile("ds_read_b64_tr_b16 %0, %1 offset:%2" : "=v"(dh[ks][nt]) : "v"(ta[2 * ks + 1]), "i"(32 * nt));
                    }
                SB();
                g = rbf(g);
                u = rbf(u);
                const float sg = rbf(g * __builtin_amdgcn_rcpf(1.0f + __expf(-g)));
                if (mw < 8) *(LAS u16*)(uintptr_t)hwo = (u16)pk(sg * u, 0.f);
            }
            bar();
            asm volatile("" : "+v"(dl[0][0]), "+v"(dl[0][1]), "+v"(dl[0][2]), "+v"(dl[0][3]), "+v"(dl[1][0]), "+v"(dl[1][1]), "+v"(dl[1][2]), "+v"(dl[1][3]),
                         "+v"(dh[0][0]), "+v"(dh[0][1]), "+v"(dh[0][2]), "+v"(dh[0][3]), "+v"(dh[1][0]), "+v"(dh[1][1]), "+v"(dh[1][2]), "+v"(dh[1][3]));
            STAMP(9);
            {
                const v4u ha0 = *(LAS v4u*)L(L_HA + (c & 7) * HAS + 16 * kq), ha1 = *(LAS v4u*)L(L_HA + (c & 7) * HAS + 16 * kq + 64);
                f4 y[4];
#pragma unroll
                for (int nt = 0; nt < 4; ++nt)
                    y[nt] = mfma(v4u{dl[1][nt].x, dl[1][nt].y, dh[1][nt].x, dh[1][nt].y}, ha1,
                                 mfma(v4u{dl[0][nt].x, dl[0][nt].y, dh[0][nt].x, dh[0][nt].y}, ha0, f4{0.f, 0.f, 0.f, 0.f}));
#pragma unroll
                for (int i = 0; i < 2; ++i) {
                    const f4 o = c < 8 ? y[2 * i] : y[2 * i + 1];
                    st128<SC1>(v4u{nos(asu(o[0])), nos(asu(o[1])), nos(asu(o[2])), nos(asu(o[3]))}, rw, po + 128 * i, 0);
                }
            }
            STAMP(10);
        } else if (isSel) {
            const int sw = mw < 4 ? mw : mw - 1;
            u32 vo[2] = {(u32)(sw * 64 + lane) * 16, (u32)(sw * 64 + lane) * 16 + 8192};
            v4u kv2[2];
            bar();
            if (mw != 4) {
                poll16<2>(rw, vo, W_KEY, kv2);
                u32 mx = max32(umax(umax(kv2[0].x, kv2[0].z), umax(kv2[1].x, kv2[1].z)));
                const auto s2 = __builtin_amdgcn_permlane32_swap(mx, mx, false, false);
                if (lane == 0) *(LAS u32*)L(L_WM + 4 * sw) = umax(s2[0], s2[1]);
            }
            STAMP(7);
            bar();
            if (mw != 4) {
                const v4u m0 = *(LAS v4u*)L(L_WM), m1 = *(LAS v4u*)L(L_WM + 16);
                const u32 thr = umin(umin(umin(m0.x, m0.y), umin(m0.z, m0.w)), umin(umin(m1.x, m1.y), umin(m1.z, m1.w)));
                u64 bm[4];
                u32 tot = 0;
#pragma unroll
                for (int i = 0; i < 4; ++i) {
                    bm[i] = __builtin_amdgcn_ballot_w64(((i & 1) ? kv2[i >> 1].z : kv2[i >> 1].x) >= thr);
                    tot += __builtin_popcountll(bm[i]);
                }
                u32 pos = 0;
                if (lane == 0) pos = __hip_atomic_fetch_add((LAS u32*)L(L_C + 4 * C_CNT), tot, __ATOMIC_RELAXED, __HIP_MEMORY_SCOPE_WORKGROUP);
                pos = __builtin_amdgcn_readfirstlane(pos);
#pragma unroll
                for (int i = 0; i < 4; ++i) {
                    const u32 key = (i & 1) ? kv2[i >> 1].z : kv2[i >> 1].x, ms = (i & 1) ? kv2[i >> 1].w : kv2[i >> 1].y;
                    const u32 at = __builtin_amdgcn_mbcnt_hi((u32)(bm[i] >> 32), __builtin_amdgcn_mbcnt_lo((u32)bm[i], pos));
                    const u32 o = key >= thr ? at * 4 : 8272u;
                    *(LAS u32*)L(L_LK + o) = key;
                    *(LAS u32*)L(L_LM + o) = ms;
                    pos += __builtin_popcountll(bm[i]);
                }
            }
            bar();
            {
                const int n = __builtin_amdgcn_readfirstlane(*(LAS u32*)L(L_C + 4 * C_CNT));
                NOUNROLL
                for (int i = tid - 64; i < n; i += 576) {
                    const u32 me = *(LAS u32*)L(L_LK + i * 4), ms = *(LAS u32*)L(L_LM + i * 4);
                    int rank = 0;
                    NOUNROLL
                    for (int j = 0; j < n; j += 32) {
                        v4u t[8];
#pragma unroll
                        for (int u = 0; u < 8; ++u) t[u] = *(LAS v4u*)L(L_LK + (j + 4 * u) * 4);
#pragma unroll
                        for (int u = 0; u < 8; ++u)
#pragma unroll
                            for (int z = 0; z < 4; ++z) rank += t[u][z] > me;
                    }
                    if (rank < 8) *(LAS v2u*)L(L_SEL + rank * 8) = v2u{me, ms};
                }
            }
            const int s = cp / 72, ch = cp - 72 * s;
            bar();
            STAMP(9);
            {
                const v2u me = *(LAS v2u*)L(L_SEL + s * 8);
                const v4u xr = ld128<0>(rq, ((2047 - me.x) & 2047) * 1152 + ch * 16, 0);
                if (cp < 4) st128<SC1>(*(LAS v4u*)L(L_SEL + cp * 16), rw, xs + cp * 16, 0);
                const float mb = lo16(me.y), inv = __builtin_amdgcn_rcpf(hi16(me.y));
                v4u o;
#pragma unroll
                for (int i = 0; i < 4; ++i) o[i] = nos(pk2((unpk(xr[i]) - mb) * inv));
                st128<SC1>(o, rw, xs + 128 + cp * 16, 0);
            }
            STAMP(10);
        } else if (HELP_GU && !isConv) {
            const rsrc_t rg = mkrs(p.wg, D * F * 2), ru = mkrs(p.wu, D * F * 2);
            v4u t[12], t3[2];
            cwait(C_GO, 0x54u);
            if (HELP_C3) {
                const u32 r = ((xi - 28) * 9 + mw) * 8 + (lane >> 3), m = (3u - xq) & 7u;
                const u32 o = (64 * (r >> 5) + (r & 31)) * (F * 2) + (9 * xq + m) * 128 + (lane & 7) * 16;
                t3[0] = ld128<0>(rg, o, 0);
                t3[1] = ld128<0>(ru, o, 0);
            }
#pragma unroll
            for (int i = 0; i < 6; ++i) {
                const u32 idx = ((xi - 28) * 9 + mw) * 64 + lane + 2304 * i, r = idx / 72, ch = idx - 72 * r;
                const u32 o = r < 180 ? pf_off(144 + r, ch, xq) : OOB;
                t[2 * i] = ld128<0>(rg, o, 0);
                t[2 * i + 1] = ld128<0>(ru, o, 0);
            }
            STAMP(7);
#pragma unroll
            for (int i = 0; i < 12; ++i) asm volatile("" ::"v"(t[i]));
            if (HELP_C3) asm volatile("" ::"v"(t3[0]), "v"(t3[1]));
            STAMP(9);
        }

        if (isK) {
            const u32 vo = cp < 288 ? cp * 4 : OOB;
            u32 qq[8];
#pragma unroll
            for (int i = 0; i < 8; ++i) qq[i] = ld32<0>(rq, vo, (8 * b + i) * 1152);
            const f2 dv = unpk(ld32<0>(mkrs(p.dv, D * 2), vo, 0));
            const v4u kvc[2] = {*(LAS v4u*)L(L_ST + cp * 16), *(LAS v4u*)L(L_ST + 5120 + cp * 16)};
            cwait_eq(C_GO, 0xFu, 4u);
            f2 xn[8], pb[8];
#pragma unroll
            for (int t = 0; t < 8; ++t) {
                const f2 st = *(LAS f2*)L(L_STAT + t * 8);
                const f2 x = unpk(qq[t]);
                xn[t] = unpk(pk2((x - st.x) * st.y));
                const u32 c0 = kvc[0][(7 - t) >> 1], c1 = kvc[1][(7 - t) >> 1];
                const f2 kv = (t & 1) ? f2{lo16(c0), lo16(c1)} : f2{hi16(c0), hi16(c1)};
                pb[t] = x + xn[t] + xn[t] * (kv * dv);
            }
            u32 vy[2] = {(u32)((cp >> 2) * 16384 + (cp & 3) * 16), (u32)(((cp + 288) >> 2) * 16384 + (cp & 3) * 16)};
#pragma unroll
            for (int t = 0; t < 8; ++t) asm volatile("" : "+v"(xn[t]), "+v"(pb[t]));
            asm volatile("" : "+v"(vy[0]), "+v"(vy[1]));
            STAMP(11);
            if (isConv) {
                bar();
                if (HELP_DN) {
                    const rsrc_t rd = mkrs(p.wd, F * D * 2), rg = mkrs(p.wg, D * F * 2), ru = mkrs(p.wu, D * F * 2);
                    v4u t[10];
                    {
                        const u32 o = cp < 288 ? pf_off(324 + 4 * xi + (u32)cp / 72, (u32)cp % 72, xq) : OOB;
                        t[0] = ld128<0>(rg, o, 0);
                        t[1] = ld128<0>(ru, o, 0);
                    }
#pragma unroll
                    for (int i = 0; i < 8; ++i) t[2 + i] = ld128<0>(rd, (cp < 288 ? cp * 16 : OOB) + 4608 * i, cj * 36864);
#pragma unroll
                    for (int i = 0; i < 10; ++i) asm volatile("" ::"v"(t[i]));
                    STAMP(9);
                }
            }
            const u32 mask = cwait<8>(C_SEL);
            u32 sr[8];
#pragma unroll
            for (int t = 0; t < 8; ++t) sr[t] = ((mask >> t) & 1u) ? OOB : (8 * b + t) * 1152;
            cwait<8>(C_F2);
            STAMP(3);
            if (isConv) {
                cwait<4>(C_YST, 4);
                if (Y_DELAY) __builtin_amdgcn_s_sleep(Y_DELAY);
            } else {
                wait_word<4>(rw, W_Y + (18 * xq + 17) * 16384 + b * 64);
            }
            STAMP(4);
            if (cp < 288) {
                v4u yv[2];
                poll16<2>(rw, vy, W_Y + b * 64, yv);
                STAMP(5);
                const rsrc_t ro = mkrs(p.out, S * D * 2);
#pragma unroll
                for (int t = 0; t < 8; ++t) {
                    const u32 c0 = yv[0][t >> 1], c1 = yv[1][t >> 1];
                    const f2 y = (t & 1) ? f2{hi16(c0), hi16(c1)} : f2{lo16(c0), lo16(c1)};
                    st32<AUX_OUT>(pk2(pb[t] + xn[t] * y), ro, vo, sr[t]);
                }
                STAMP(6);
            }
        }
    }
    {
        u32 extra = 0;
        if (PROBE_COND) extra = 10 * (__builtin_amdgcn_readfirstlane(ts[PROBE_B]) - ts[PROBE_A]);
        if (extra > 30000) extra = 30000;
        while (__builtin_amdgcn_s_memrealtime() - pad0 < PADUS * 100 + extra) __builtin_amdgcn_s_sleep(1);
    }
}

extern "C" void kernel_launch(void* const* d_in, const int* in_sizes, int n_in, void* d_out, int out_size, void* d_ws, size_t ws_size,
                              hipStream_t stream) {
    Params p;
    p.q = (const u16*)d_in[0];
    p.k = (const u16*)d_in[1];
    p.v = (const u16*)d_in[2];
    p.sk = (const u16*)d_in[3];
    p.h = (const u16*)d_in[4];
    p.dv = (const u16*)d_in[5];
    p.wr = (const u16*)d_in[7];
    p.wg = (const u16*)d_in[8];
    p.wu = (const u16*)d_in[9];
    p.wd = (const u16*)d_in[10];
    p.out = (u16*)d_out;
    p.ws = (char*)d_ws + (WS_AT_END ? ((ws_size - WS_BYTES) & ~(size_t)4095) : 0);
    fused_kernel<<<256, NT, 0, stream>>>(p);
}
```

```cpp
#define PADUS 40
#define PROBE_A 0
#define PROBE_B 8
#define PROBE_COND (isMoe&&isL)
#define Moe&&wave =0:4
#include <hip/hip_runtime.h>
#include <stdint.h>

typedef unsigned short u16;
typedef unsigned u32;
typedef unsigned long long u64;
typedef u32 v4u __attribute__((ext_vector_type(4)));
typedef u32 v2u __attribute__((ext_vector_type(2)));
typedef float f4 __attribute__((ext_vector_type(4)));
typedef float f2 __attribute__((ext_vector_type(2)));
typedef __bf16 bf8 __attribute__((ext_vector_type(8)));
typedef __bf16 bf2 __attribute__((ext_vector_type(2)));
#define GAS __attribute__((address_space(1)))
#define LAS __attribute__((address_space(3)))
typedef __amdgpu_buffer_rsrc_t rsrc_t;

#define AUX_IN 0
#define CLR_AUX 16
#define AUX_OUT 16
#define WS_AT_END 1
#define H_DELAY 0
#define Q_DELAY 4
#define U_DELAY 6
#define F_DELAY 36
#define K_DELAY 14
#define Y_DELAY 8
#define YDIRECT 0
#define X_DELAY 6
#define CODE_KB 29
#define HELP_C3 1
#define PF1 2
#define SPRIO 1
#define NP1 2
#define NP2 4
#define HELP_GU 1
#define HELP_DN 1

constexpr int S = 2048, D = 576, F = 4608;
constexpr int NT = 640;
constexpr u32 SENT = 0xFFFFFFFFu;
constexpr int SC1 = 16;
constexpr u32 SPIN_CAP = 1u << 16;
constexpr u32 OOB = 0x40000000u;

constexpr int XSB = 9344;
constexpr int W_E = 0, W_F3 = 1024, W_F1 = 4096, W_F2 = W_F1 + 2048, W_KEY = W_F2 + 2048, W_XS = W_KEY + 256 * 64, W_U = W_XS + 8 * XSB,
              W_Y = W_U + 256 * 9216, W_PART = W_Y + 144 * 16384, W_END = W_PART + 72 * 18432, W_DBG = W_END;
constexpr int WS_BYTES = W_END + (1 << 20);
static_assert(W_U % 128 == 0 && XSB % 128 == 0, "align");
constexpr int L_C = 0, L_STAT = L_C + 64, L_SEL = L_STAT + 64, L_WM = L_SEL + 64, L_RED = L_WM + 64, L_ST = 512, L_SQ = L_ST + 320 * 32, L_RA = L_SQ + 320 * 36 + 256;
constexpr int HC = 4160, HCH = 4 * HC, L_H = L_RA, UCH = 290 * 16, L_U = L_H + 4 * HCH, L_Y = L_U + 4 * UCH;
constexpr int L_R = L_RA, L_HA = L_R + 9 * 8 * 128 * 4, HAS = 144, DNS = 1056, L_DN = L_HA + 1280, L_DT = L_DN + 64 * DNS, L_XS = L_DT + 8192,
              XSS = 1168, L_END = L_XS + 8 * XSS;
constexpr int L_LK = L_RA, L_RK = L_LK + 512;
static_assert(L_Y + 16384 <= L_END && L_RA % 256 == 0 && L_DN % 256 == 0, "lds");
enum { C_CLR = 0, C_F1 = 1, C_F2 = 2, C_SEL = 3, C_UST = 4, C_CNT = 5, C_ST = 6, C_YST = 7, C_WI = 8, C_GO = 9, C_XS = 10, C_YL = 11, C_DN = 12 };

struct Params {
    const u16 *q, *k, *v, *sk, *h, *dv, *wr, *wg, *wu, *wd;
    u16* out;
    char* ws;
};

__device__ __forceinline__ float asf(u32 x) { return __builtin_bit_cast(float, x); }
__device__ __forceinline__ u32 asu(float x) { return __builtin_bit_cast(u32, x); }
__device__ __forceinline__ float lo16(u32 x) { return asf(x << 16); }
__device__ __forceinline__ float hi16(u32 x) { return asf(x & 0xFFFF0000u); }
__device__ __forceinline__ f2 unpk(u32 x) { return f2{lo16(x), hi16(x)}; }
__device__ __forceinline__ u32 pk(float a, float b) { return __builtin_bit_cast(u32, __builtin_convertvector(f2{a, b}, bf2)); }
__device__ __forceinline__ u32 pk2(f2 a) { return pk(a.x, a.y); }
__device__ __forceinline__ float rbf(float a) { return lo16(pk(a, a)); }
__device__ __forceinline__ u32 nos(u32 x) { return x < 0xFFFFFFFEu ? x : 0xFFFFFFFEu; }
__device__ __forceinline__ u32 umax(u32 a, u32 b) { return a > b ? a : b; }
__device__ __forceinline__ u32 umin(u32 a, u32 b) { return a < b ? a : b; }
__device__ __forceinline__ bool valid4(v4u v) { return umax(umax(v.x, v.y), umax(v.z, v.w)) != SENT; }
__device__ __forceinline__ rsrc_t mkrs(const void* p, u32 bytes) { return __builtin_amdgcn_make_buffer_rsrc((void*)p, 0, bytes, 0x00020000); }
template <int AUX> __device__ __forceinline__ u32 ld32(rsrc_t r, u32 vo, u32 so) { return __builtin_amdgcn_raw_buffer_load_b32(r, vo, so, AUX); }
template <int AUX> __device__ __forceinline__ v2u ld64(rsrc_t r, u32 vo, u32 so) { return __builtin_amdgcn_raw_buffer_load_b64(r, vo, so, AUX); }
template <int AUX> __device__ __forceinline__ v4u ld128(rsrc_t r, u32 vo, u32 so) { return __builtin_amdgcn_raw_buffer_load_b128(r, vo, so, AUX); }
template <int AUX> __device__ __forceinline__ void st32(u32 v, rsrc_t r, u32 vo, u32 so) { __builtin_amdgcn_raw_buffer_store_b32(v, r, vo, so, AUX); }
template <int AUX> __device__ __forceinline__ void st64(v2u v, rsrc_t r, u32 vo, u32 so) { __builtin_amdgcn_raw_buffer_store_b64(v, r, vo, so, AUX); }
template <int AUX> __device__ __forceinline__ void st128(v4u v, rsrc_t r, u32 vo, u32 so) { __builtin_amdgcn_raw_buffer_store_b128(v, r, vo + so, 0, AUX); }
#define FENCE() asm volatile("" ::: "memory")
#define DRAIN() asm volatile("s_waitcnt vmcnt(0)" ::: "memory")
#define LGKM0() asm volatile("s_waitcnt lgkmcnt(0)" ::: "memory")
#define SB() __builtin_amdgcn_sched_barrier(0)
#define NOUNROLL _Pragma("unroll 1")
__device__ __forceinline__ void bar() {
    SB();
    LGKM0();
    __builtin_amdgcn_s_barrier();
    FENCE();
    SB();
}
template <int CTRL> __device__ __forceinline__ u32 dpp(u32 x) { return (u32)__builtin_amdgcn_update_dpp(0, (int)x, CTRL, 0xF, 0xF, true); }
__device__ __forceinline__ float sum32(float x) {
    x += asf(dpp<0xB1>(asu(x)));
    x += asf(dpp<0x4E>(asu(x)));
    x += asf(dpp<0x141>(asu(x)));
    x += asf(dpp<0x140>(asu(x)));
    auto r = __builtin_amdgcn_permlane16_swap(asu(x), asu(x), false, false);
    return asf(r[0]) + asf(r[1]);
}
__device__ __forceinline__ u32 max32(u32 x) {
    x = umax(x, dpp<0xB1>(x));
    x = umax(x, dpp<0x4E>(x));
    x = umax(x, dpp<0x141>(x));
    x = umax(x, dpp<0x140>(x));
    auto r = __builtin_amdgcn_permlane16_swap(x, x, false, false);
    return umax(r[0], r[1]);
}
__device__ __forceinline__ f4 mfma(v4u a, v4u b, f4 c) {
    return __builtin_amdgcn_mfma_f32_16x16x32_bf16(__builtin_bit_cast(bf8, a), __builtin_bit_cast(bf8, b), c, 0, 0, 0);
}

#define STAMP(i) do { if ((i) == PROBE_A || (i) == PROBE_B) { SB(); ts[i] = (u32)__builtin_amdgcn_s_memrealtime(); SB(); } } while (0)

__shared__ __attribute__((aligned(16))) char lds_raw[L_END];

__device__ __forceinline__ LAS char* L(int off) { return (LAS char*)lds_raw + off; }
__device__ __forceinline__ void cset(int w, u32 v = 1u) { *(volatile LAS u32*)L(L_C + 4 * w) = v; }
template <int SL = 1> __device__ __forceinline__ u32 cwait(int w, u32 want = 1u) {
    u32 x = 0;
    NOUNROLL
    for (u32 n = 0; n < (1u << 22); ++n) {
        x = __builtin_amdgcn_readfirstlane(*(volatile LAS u32*)L(L_C + 4 * w));
        if (x >= want) break;
        if (SL) __builtin_amdgcn_s_sleep(SL);
    }
    FENCE();
    return x;
}
__device__ __forceinline__ u32 cpeek(int w) { return *(volatile LAS u32*)L(L_C + 4 * w); }
__device__ __forceinline__ void cadd(int w, u32 v = 1u) {
    FENCE();
    if ((threadIdx.x & 63) == 0) __hip_atomic_fetch_add((LAS u32*)L(L_C + 4 * w), v, __ATOMIC_RELAXED, __HIP_MEMORY_SCOPE_WORKGROUP);
}
template <int SL = 1> __device__ __forceinline__ void cwait_eq(int w, u32 mask, u32 want) {
    NOUNROLL
    for (u32 n = 0; n < (1u << 22); ++n) {
        if ((__builtin_amdgcn_readfirstlane(*(volatile LAS u32*)L(L_C + 4 * w)) & mask) == want) break;
        if (SL) __builtin_amdgcn_s_sleep(SL);
    }
    FENCE();
}
__device__ __forceinline__ void wait_flags(rsrc_t rw, int base, u32 tlo, u32 thi, int lane) {
    NOUNROLL
    for (u32 n = 0; n < SPIN_CAP; ++n) {
        FENCE();
        const v4u f0 = ld128<SC1>(rw, lane * 16, base), f1 = ld128<SC1>(rw, lane * 16 + 1024, base);
        if (__all((((f0.x ^ tlo) | (f0.z ^ tlo) | (f1.x ^ tlo) | (f1.z ^ tlo)) | ((f0.y ^ thi) | (f0.w ^ thi) | (f1.y ^ thi) | (f1.w ^ thi))) == 0u)) break;
        __builtin_amdgcn_s_sleep(8);
    }
    FENCE();
}
__device__ __forceinline__ void wait_flag(rsrc_t rw, int off, u32 tlo, u32 thi) {
    NOUNROLL
    for (u32 n = 0; n < SPIN_CAP; ++n) {
        FENCE();
        const v2u f = ld64<SC1>(rw, 0, off);
        if (f.x == tlo && f.y == thi) break;
        __builtin_amdgcn_s_sleep(4);
    }
    FENCE();
}
template <int SL> __device__ __forceinline__ void wait_word(rsrc_t rw, u32 off) {
    NOUNROLL
    for (u32 n = 0; n < SPIN_CAP; ++n) {
        FENCE();
        if (ld32<SC1>(rw, 0, off) != SENT) break;
        __builtin_amdgcn_s_sleep(SL);
    }
    FENCE();
}
template <int N> __device__ __forceinline__ void poll_issue(rsrc_t rw, const u32 (&vo)[N], u32 so, v4u (&v)[N]) {
    FENCE();
#pragma unroll
    for (int i = 0; i < N; ++i) v[i] = ld128<SC1>(rw, vo[i], so);
    SB();
}
template <int N> __device__ __forceinline__ void poll_finish(rsrc_t rw, const u32 (&vo)[N], u32 so, v4u (&v)[N]) {
    NOUNROLL
    for (u32 n = 0; n < SPIN_CAP; ++n) {
        bool ok[N], all = true;
#pragma unroll
        for (int i = 0; i < N; ++i) { ok[i] = valid4(v[i]); all &= ok[i]; }
        if (__all(all)) break;
        FENCE();
#pragma unroll
        for (int i = 0; i < N; ++i) if (!ok[i]) v[i] = ld128<SC1>(rw, vo[i], so);
    }
    FENCE();
}
template <int N> __device__ __forceinline__ void poll16(rsrc_t rw, const u32 (&vo)[N], u32 so, v4u (&v)[N]) {
    poll_issue<N>(rw, vo, so, v);
    poll_finish<N>(rw, vo, so, v);
}
__device__ __forceinline__ u32 selmask(int b) {
    const int lane = threadIdx.x & 63;
    const u32 rel = (2047u - (*(LAS u32*)L(L_SEL + (lane & 7) * 8) & 0xFFFFu)) - 8u * b;
    u32 m = rel < 8u ? 1u << rel : 0u;
    m |= dpp<0xB1>(m);
    m |= dpp<0x4E>(m);
    m |= dpp<0x141>(m);
    return m;
}

#define DSR64(dst, addr, off) asm volatile("ds_read_b64 %0, %1 offset:%2" : "=v"(dst) : "v"(addr), "i"(off))
#define DSR128(dst, addr, off) asm volatile("ds_read_b128 %0, %1 offset:%2" : "=v"(dst) : "v"(addr), "i"(off))
#define MFMA_IP(acc, a, b) asm volatile("v_mfma_f32_16x16x32_bf16 %0, %1, %2, %0" : "+v"(acc) : "v"(a), "v"(b))

struct AF { v2u lo, hi; };
__device__ __forceinline__ v4u cat(const AF& a) { return v4u{a.lo.x, a.lo.y, a.hi.x, a.hi.y}; }

template <int I0> __device__ __forceinline__ void conv_phase(u32 va, u32 vb, f4 (&acc)[8], AF (&alo)[4], v4u (&blo)[4], AF (&alo2)[4], v4u (&blo2)[4]) {
    AF ahi[4];
    v4u bhi[4];
    constexpr int OA = 64 * I0, OB = 448 - 64 * I0;
    asm volatile("s_waitcnt lgkmcnt(0)" : "+v"(alo[0].lo), "+v"(alo[0].hi), "+v"(alo[1].lo), "+v"(alo[1].hi), "+v"(alo[2].lo), "+v"(alo[2].hi), "+v"(alo[3].lo),
                 "+v"(alo[3].hi), "+v"(blo[0]), "+v"(blo[1]), "+v"(blo[2]), "+v"(blo[3]));
    SB();
#pragma unroll
    for (int n = 0; n < 16; ++n) {
        const int e = n >> 2, b = n & 3;
        MFMA_IP(acc[e + b], cat(alo[e]), blo[b]);
        if (n < 4) DSR128(bhi[n], vb, OB + 512 * (4 + n));
        else if (n < 12) {
            if (n & 1) DSR64(ahi[(n - 4) >> 1].hi, va, OA + 512 * (4 + ((n - 4) >> 1)) + 8);
            else DSR64(ahi[(n - 4) >> 1].lo, va, OA + 512 * (4 + ((n - 4) >> 1)));
        }
        SB();
    }
    asm volatile("s_waitcnt lgkmcnt(0)" : "+v"(ahi[0].lo), "+v"(ahi[0].hi), "+v"(ahi[1].lo), "+v"(ahi[1].hi), "+v"(ahi[2].lo), "+v"(ahi[2].hi), "+v"(ahi[3].lo),
                 "+v"(ahi[3].hi), "+v"(bhi[0]), "+v"(bhi[1]), "+v"(bhi[2]), "+v"(bhi[3]));
    SB();
    int n = 0;
#pragma unroll
    for (int half = 0; half < 2; ++half)
#pragma unroll
        for (int e = 0; e < 4; ++e)
#pragma unroll
            for (int b = 0; b < 4 - e; ++b) {
                if (half == 0) MFMA_IP(acc[4 + e + b], cat(alo[e]), bhi[b]);
                else MFMA_IP(acc[4 + e + b], cat(ahi[e]), blo[b]);
                if (I0 < 7) {
                    if (n < 4) DSR128(blo2[n], vb, OB - 64 + 512 * n);
                    else if (n < 12) {
                        if (n & 1) DSR64(alo2[(n - 4) >> 1].hi, va, OA + 64 + 512 * ((n - 4) >> 1) + 8);
                        else DSR64(alo2[(n - 4) >> 1].lo, va, OA + 64 + 512 * ((n - 4) >> 1));
                    }
                }
                ++n;
                SB();
            }
}

__device__ __forceinline__ u32 pf_off(u32 idx, u32 ch, int xq) {
    const u32 g = idx / 36u, rest = idx - 36u * g;
    const u32 row = 64u * (rest >> 2) + 8u * (rest & 3u) + 32u * (g >> 3) + (g & 7u);
    return row * (F * 2) + xq * 1152 + ch * 16;
}

template <class TS>
__device__ __forceinline__ void ln_part(rsrc_t rw, int b, int lw, int lane, const u32 (&xq9)[11], const u32 (&wr9)[9], TS& ts) {
    const int hf = lane >> 5, l32 = lane & 31, tl = 2 * lw + hf, tok = 8 * b + tl;
    u32 ko = l32 == 0 ? W_KEY + b * 64 + tl * 8 : OOB;
    asm volatile("" : "+v"(ko));
    asm volatile("" ::"v"(xq9[8]));
    STAMP(10);
    f2 x[9];
    f2 s2 = {0.f, 0.f};
#pragma unroll
    for (int i = 0; i < 9; ++i) { x[i] = unpk(xq9[i]); s2 += x[i]; }
    const float mean = sum32(s2.x + s2.y) * (1.0f / D);
    f2 v2 = {0.f, 0.f};
#pragma unroll
    for (int i = 0; i < 9; ++i) { const f2 dl = x[i] - mean; v2 += dl * dl; }
    const u32 clr = cpeek(C_CLR);
    const float var = rbf(sum32(v2.x + v2.y) * (1.0f / D));
    const float mb = rbf(mean), sd = rbf(__builtin_amdgcn_sqrtf(rbf(var + 1e-5f)));
    const float inv = __builtin_amdgcn_rcpf(sd);
    float a0 = 0.f, a1 = 0.f;
#pragma unroll
    for (int i = 0; i < 9; ++i) {
        const u32 xn = pk2((x[i] - mb) * inv);
        if (i & 1) a1 = __builtin_amdgcn_fdot2_f32_bf16(__builtin_bit_cast(bf2, xn), __builtin_bit_cast(bf2, wr9[i]), a1, false);
        else a0 = __builtin_amdgcn_fdot2_f32_bf16(__builtin_bit_cast(bf2, xn), __builtin_bit_cast(bf2, wr9[i]), a0, false);
    }
    const float rs = sum32(a0 + a1);
    const u32 rb = pk(rs, rs) & 0xFFFFu;
    u32 ord = (rb & 0x8000u) ? (~rb & 0xFFFFu) : (rb | 0x8000u);
    if ((rb & 0x7FFFu) > 0x7F80u) ord = 0u;
    v2u rec = {(ord << 16) | (u32)(2047 - tok), nos(pk(mb, sd))};
    asm volatile("" : "+v"(rec));
    STAMP(1);
    if (!__builtin_amdgcn_readfirstlane(clr)) cwait(C_CLR);
    st64<SC1>(rec, rw, ko, 0);
    if (l32 == 0) *(LAS f2*)L(L_STAT + tl * 8) = f2{mb, inv};
    cadd(C_GO);
    STAMP(2);
}

typedef u32 v16u __attribute__((ext_vector_type(16)));
typedef u32 v8u __attribute__((ext_vector_type(8)));
__device__ __forceinline__ u64 mk64(u32 lo, u32 hi) { return ((u64)hi << 32) | lo; }

__global__ void __launch_bounds__(NT, 3) fused_kernel(Params) {
    v16u ka;
    v8u kb;
    asm volatile("s_load_dwordx16 %0, %2, 0x0\n\ts_load_dwordx8 %1, %2, 0x40" : "=&s"(ka), "=&s"(kb) : "s"(__builtin_amdgcn_kernarg_segment_ptr()));
    const int tid = threadIdx.x, lane = tid & 63, wave = __builtin_amdgcn_readfirstlane(tid >> 6);
    const int wg = blockIdx.x, xq = wg & 7, xi = wg >> 3, b = 32 * xq + xi;
    const bool isConv = xi < 18, isMoe = xi >= 18 && xi < 27, isSel = xi == 27;
    const int cj = 18 * xq + xi, mu = 9 * xq + xi - 18;
    const u32 xs = W_XS + xq * XSB;
    u32 ts[12];
#pragma unroll
    for (int i = 0; i < 12; ++i) ts[i] = 0;
    u64 pc;
    asm volatile("s_getpc_b64 %0" : "=s"(pc));
    STAMP(0);
    const u64 pad0 = __builtin_amdgcn_s_memrealtime();
    if (SPRIO && wave == 0) __builtin_amdgcn_s_setprio(3);
    const bool isK = wave >= 1 && wave < 6, isL = wave >= 6;
    const int cp = tid - 64;
    const int lw = wave - 6;
    const int mw = wave - 1;
    u32 so[12], nsk = isK ? D * 8 : 0, nv = isK ? S * D * 2 : (isL ? 384 + D * 2 : 0);
    {
        u32 s0 = isL ? 0u : (u32)(8 * b - 3) * 1152u, st = isL ? 128u : 1152u;
        asm volatile("" : "+s"(s0), "+s"(st), "+s"(nsk), "+s"(nv));
#pragma unroll
        for (int i = 0; i < 12; ++i) {
            so[i] = s0 + i * st;
            asm volatile("" : "+s"(so[i]));
        }
    }
    u32 va0 = (u32)cp < 288u ? cp * 4 : OOB, va1 = (8 * b + 2 * lw + (lane >> 5)) * 1152 + (lane & 31) * 4;
    asm volatile("" : "+v"(va0), "+v"(va1));
    u32 vb0 = tid >= 384 ? (lane & 31) * 4 : va0, vb1 = tid >= 384 ? (lane & 31) * 4 : OOB;
    va0 = tid >= 384 ? va1 : va0;
    asm volatile("" : "+v"(va0), "+v"(vb0), "+v"(vb1));
    SB();
    v4u pf = {0u, 0u, 0u, 0u};
    if (isL) {
        if (CODE_KB && xi * 4 + lw < CODE_KB) pf = *(const GAS v4u*)((pc & ~255ull) + (xi * 4 + lw) * 1024 + lane * 16);
        if (Q_DELAY) __builtin_amdgcn_s_sleep(Q_DELAY);
    }
    SB();
    asm volatile("s_waitcnt lgkmcnt(0)" : "+s"(ka), "+s"(kb));
    Params p;
    p.q = (const u16*)mk64(ka[0], ka[1]);
    p.k = (const u16*)mk64(ka[2], ka[3]);
    p.v = (const u16*)mk64(ka[4], ka[5]);
    p.sk = (const u16*)mk64(ka[6], ka[7]);
    p.h = (const u16*)mk64(ka[8], ka[9]);
    p.dv = (const u16*)mk64(ka[10], ka[11]);
    p.wr = (const u16*)mk64(ka[12], ka[13]);
    p.wg = (const u16*)mk64(ka[14], ka[15]);
    p.wu = (const u16*)mk64(kb[0], kb[1]);
    p.wd = (const u16*)mk64(kb[2], kb[3]);
    p.out = (u16*)mk64(kb[4], kb[5]);
    p.ws = (char*)mk64(kb[6], kb[7]);
    const rsrc_t rw = mkrs(p.ws, WS_BYTES);
    const rsrc_t rq = mkrs(p.q, S * D * 2);

    u64 pa = isL ? (u64)p.q : (u64)p.k, pb = isL ? (u64)p.wr - 384 : (u64)p.v;
    asm volatile("" : "+s"(pa), "+s"(pb));
    const v4u s4 = {SENT, SENT, SENT, SENT};
    u32 ra[11], rb[9];
    v4u sk;
    if (wave != 0) {
        sk = ld128<0>(mkrs(p.sk, nsk), cp * 16, 0);
        const rsrc_t da = mkrs((const void*)pa, S * D * 2), db = mkrs((const void*)pb, nv);
#pragma unroll
        for (int i = 0; i < 11; ++i) {
            ra[i] = ld32<AUX_IN>(da, va0, so[i]);
            if (i >= 3) rb[i - 3] = ld32<AUX_IN>(db, vb0, so[i]);
        }
        rb[8] = ld32<AUX_IN>(db, vb1, so[11]);
    }
    SB();
    v2u e = {0u, 0u};
    if (wave == 0) {
#pragma unroll
        for (int i = 0; i < 9; ++i) st128<SC1>(s4, rw, lane * 16 + 1024 * i, W_U + b * 9216);
        st128<SC1>(s4, rw, lane < 4 ? W_KEY + b * 64 + lane * 16 : OOB, 0);
        e = ld64<SC1>(rw, 0, W_E);
        if (lane < 16) *(LAS u32*)L(L_C + 4 * lane) = 0u;
    }
    STAMP(11);
    bar();
    STAMP(8);

    if (wave == 0) {
        if (SPRIO) __builtin_amdgcn_s_setprio(0);
        DRAIN();
        const u64 tag = (((u64)e.y << 32) | e.x) + 1ull;
        const u32 tlo = __builtin_amdgcn_readfirstlane((u32)tag), thi = __builtin_amdgcn_readfirstlane((u32)(tag >> 32));
        if (lane == 0) st64<SC1>(v2u{tlo, thi}, rw, 0, W_F1 + wg * 8);
        cset(C_CLR);
        STAMP(1);
        if (isMoe) {
#pragma unroll
            for (int i = 0; i < 18; ++i) st128<SC1>(s4, rw, lane * 16 + 1024 * i, W_PART + mu * 18432);
            u32 vo[10], xw[9];
#pragma unroll
            for (int i = 0; i < 9; ++i) {
                const int c = lane + 64 * i;
                vo[i] = 128 + c * 16;
                xw[i] = (u32)(uintptr_t)L(L_XS + (c / 72) * XSS + (c % 72) * 16);
                asm volatile("" : "+v"(vo[i]), "+v"(xw[i]));
            }
            vo[9] = (lane & 3) * 16;
            cwait(C_GO, 0x54u);
            STAMP(2);
            {
                const v4u sd = {(u32)(u64)p.wd, (u32)((u64)p.wd >> 32) & 0xFFFFu, (u32)(F * D * 2), 0x00020000u};
                u32 ro = 64 * mu * 1152;
                asm volatile("s_mov_b32 m0, %2\n\ts_nop 0\n\t.rept 64\n\tbuffer_load_dwordx4 %1, %3, %0 offen lds\n\ts_add_u32 m0, m0, 1056\n\t"
                             "s_add_u32 %0, %0, 1152\n\t.endr"
                             : "+s"(ro) : "v"(lane * 16), "s"((u32)(uintptr_t)L(L_DN)), "s"(sd) : "memory", "scc");
                ro = 64 * mu * 1152;
                asm volatile("s_mov_b32 m0, %2\n\ts_nop 0\n\t.rept 8\n\tbuffer_load_dwordx4 %1, %3, %0 offen lds\n\ts_add_u32 m0, m0, 1024\n\t"
                             "s_add_u32 %0, %0, 9216\n\t.endr"
                             : "+s"(ro) : "v"((lane >> 3) * 1152 + 1024 + (lane & 7) * 16), "s"((u32)(uintptr_t)L(L_DT)), "s"(sd) : "memory", "scc");
            }
            cset(C_WI);
            STAMP(3);
            DRAIN();
            cset(C_DN);
            if (lane == 0) st64<SC1>(v2u{tlo, thi}, rw, 0, W_F2 + wg * 8);
            STAMP(4);
            wait_flag(rw, W_F3 + xq * 128, tlo, thi);
            wait_word<2>(rw, xs + 60);
            if (X_DELAY) __builtin_amdgcn_s_sleep(X_DELAY);
            STAMP(5);
            v4u xv[10];
            poll16<10>(rw, vo, xs, xv);
#pragma unroll
            for (int i = 0; i < 9; ++i) *(LAS v4u*)(uintptr_t)xw[i] = xv[i];
            if (lane < 4) *(LAS v4u*)L(L_SEL + lane * 16) = xv[9];
            STAMP(6);
            bar();
            cset(C_SEL, 0x100u | selmask(b));
            bar();
            bar();
            wait_flags(rw, W_F2, tlo, thi, lane);
            cset(C_F2);
        } else if (isSel) {
#pragma unroll
            for (int i = 0; i < 9; ++i) st128<SC1>(s4, rw, lane * 16 + 1024 * i, xs + 128);
            st128<SC1>(s4, rw, lane < 4 ? xs + lane * 16 : OOB, 0);
            DRAIN();
            if (lane == 0) {
                st64<SC1>(v2u{tlo, thi}, rw, 0, W_F2 + wg * 8);
                st64<SC1>(v2u{tlo, thi}, rw, 0, W_F3 + xq * 128);
            }
            __builtin_amdgcn_s_sleep(F_DELAY > 16 ? F_DELAY - 16 : 0);
            wait_flags(rw, W_F1, tlo, thi, lane);
            STAMP(2);
            bar();
            if (xq == 0 && lane == 0) st64<SC1>(v2u{tlo, thi}, rw, 0, W_E);
            bar();
            bar();
            bar();
            cwait(C_XS);
            cset(C_SEL, 0x100u | selmask(b));
            __builtin_amdgcn_s_sleep(32);
            wait_flags(rw, W_F2, tlo, thi, lane);
            cset(C_F2);
        } else {
            if (isConv) {
                if (F_DELAY) __builtin_amdgcn_s_sleep(F_DELAY);
                wait_flags(rw, W_F1, tlo, thi, lane);
                cset(C_F1);
                STAMP(2);
                __builtin_amdgcn_s_setprio(3);
                bar();
#pragma unroll
                for (int i = 0; i < 16; ++i) st128<SC1>(s4, rw, lane * 16 + 1024 * i, W_Y + cj * 16384);
                __builtin_amdgcn_s_setprio(0);
                DRAIN();
                cadd(C_YL);
                if (lane == 0) st64<SC1>(v2u{tlo, thi}, rw, 0, W_F2 + wg * 8);
            } else {
                if (lane == 0) st64<SC1>(v2u{tlo, thi}, rw, 0, W_F2 + wg * 8);
                cwait<4>(C_GO, 0x54u);
                __builtin_amdgcn_s_sleep(32);
            }
            wait_flag(rw, W_F3 + xq * 128, tlo, thi);
            STAMP(3);
            u32 vo[1] = {(u32)(lane & 3) * 16};
            v4u sv[1];
            wait_word<16>(rw, xs + 60);
            poll16<1>(rw, vo, xs, sv);
            if (lane < 4) *(LAS v4u*)L(L_SEL + lane * 16) = sv[0];
            LGKM0();
            cset(C_SEL, 0x100u | selmask(b));
            STAMP(4);
            wait_flags(rw, W_F2, tlo, thi, lane);
            cset(C_F2);
            STAMP(5);
            if (isConv) {
                const int s = cj / 18, cb = cj % 18, d = 32 * cb + (lane & 31);
                const v2u me = *(LAS v2u*)L(L_SEL + s * 8);
                const int tok = (2047 - me.x) & 2047;
                const u32 ord = me.x >> 16, rb = (ord & 0x8000u) ? (ord & 0x7FFFu) : (~ord & 0xFFFFu);
                const float mb = lo16(me.y), inv = __builtin_amdgcn_rcpf(hi16(me.y));
                const u32 eo = (tok * D + d) * 2;
                const rsrc_t rk = mkrs(p.k, S * D * 2);
                const u32 xb = __builtin_amdgcn_raw_buffer_load_b16(rq, eo, 0, 0), vb = __builtin_amdgcn_raw_buffer_load_b16(mkrs(p.v, S * D * 2), eo, 0, 0);
                u32 kb[4];
#pragma unroll
                for (int j = 0; j < 4; ++j) kb[j] = __builtin_amdgcn_raw_buffer_load_b16(rk, tok >= j ? eo - j * 1152 : OOB, 0, 0);
                const v2u sw = ld64<0>(mkrs(p.sk, D * 8), d * 8, 0);
                const u32 db = __builtin_amdgcn_raw_buffer_load_b16(mkrs(p.dv, D * 2), d * 2, 0, 0);
                const float x = lo16(xb);
                const float kv = rbf((lo16(kb[3]) * hi16(sw.y) + lo16(kb[2]) * lo16(sw.y) + lo16(kb[1]) * hi16(sw.x) + lo16(kb[0]) * lo16(sw.x)) * lo16(vb));
                const float xn = rbf((x - mb) * inv);
                float base = x + xn * kv * lo16(db);
                float rsg = rbf(__builtin_amdgcn_rcpf(1.0f + __expf(-lo16(rb))));
                const int c = (d & 1) * 288 + (d >> 1);
                u32 yo = (c >> 2) * 16384 + (c & 3) * 16 + (tok >> 3) * 64 + ((tok & 7) >> 1) * 4;
                u32 vp[9];
#pragma unroll
                for (int i = 0; i < 9; ++i) vp[i] = (8 * i + (lane >> 3)) * 18432 + (lane & 7) * 16;
                asm volatile("" : "+v"(base), "+v"(rsg), "+v"(yo));
                wait_word<4>(rw, W_PART + (cj % 72) * 18432 + s * 2304 + cb * 128);
                cwait<2>(C_YST, 4);
                if (Y_DELAY) __builtin_amdgcn_s_sleep(Y_DELAY);
                STAMP(6);
                v4u pv[9];
                u32 yw = ld32<SC1>(rw, yo, W_Y);
                poll16<9>(rw, vp, W_PART + s * 2304 + cb * 128, pv);
                STAMP(7);
                f4 a = {0.f, 0.f, 0.f, 0.f};
#pragma unroll
                for (int i = 0; i < 9; ++i) a += __builtin_bit_cast(f4, pv[i]);
#pragma unroll
                for (int i = 0; i < 4; ++i) {
                    float t = a[i];
                    t += asf(dpp<0x128>(asu(t)));
                    const auto r1 = __builtin_amdgcn_permlane16_swap(asu(t), asu(t), false, false);
                    t = asf(r1[0]) + asf(r1[1]);
                    const auto r2 = __builtin_amdgcn_permlane32_swap(asu(t), asu(t), false, false);
                    a[i] = asf(r2[0]) + asf(r2[1]);
                }
                if (lane < 8) *(LAS f4*)L(L_RED + lane * 16) = a;
                LGKM0();
                base += rbf(xn + rbf(rbf(*(LAS float*)L(L_RED + (lane & 31) * 4)) * rsg));
                asm volatile("" : "+v"(base));
                STAMP(8);
                NOUNROLL
                for (u32 n = 0; n < SPIN_CAP; ++n) {
                    if (__all(yw != SENT)) break;
                    FENCE();
                    yw = ld32<SC1>(rw, yo, W_Y);
                }
                const float o = base + xn * ((tok & 1) ? hi16(yw) : lo16(yw));
                if (lane < 32) __builtin_amdgcn_raw_buffer_store_b16((u16)pk(o, 0.f), mkrs(p.out, S * D * 2), eo, 0, AUX_OUT);
                STAMP(9);
            }
        }
    } else {
        v4u t1[2 * NP1];
#pragma unroll
        for (int i = 0; i < 2 * NP1; ++i) t1[i] = s4;
        u32 qq[8], dvp;
        if (isK) {
            u32 uo = cp < 288 ? W_U + b * 9216 + cp * 16 : OOB;
            u32 pfo[NP1];
#pragma unroll
            for (int i = 0; i < NP1; ++i) pfo[i] = cp < 288 ? pf_off(4 * (18 * i + xi) + (u32)cp / 72, (u32)cp % 72, xq) : OOB;
            asm volatile("" : "+v"(uo));
#pragma unroll
            for (int i = 0; i < NP1; ++i) asm volatile("" : "+v"(pfo[i]));
            const f2 w0 = {lo16(sk.x), lo16(sk.z)}, w1 = {hi16(sk.x), hi16(sk.z)}, w2 = {lo16(sk.y), lo16(sk.w)}, w3 = {hi16(sk.y), hi16(sk.w)};
            f2 kf[11], r[8];
            asm volatile("" ::"v"(rb[7]));
            STAMP(10);
            SB();
            v4u hd[4];
            {
                const int c = 4 * cj + mw;
                const rsrc_t rh = mkrs(p.h, isConv && mw < 4 ? D * S * 2 : 0);
#pragma unroll
                for (int i = 0; i < 4; ++i) hd[i] = ld128<AUX_IN>(rh, lane * 16 + 1024 * i, (c < 288 ? 2 * c : 2 * (c - 288) + 1) * 4096);
            }
            SB();
#pragma unroll
            for (int i = 0; i < 11; ++i) kf[i] = unpk(ra[i]);
#pragma unroll
            for (int t = 0; t < 8; ++t) r[t] = (kf[t] * w3 + kf[t + 1] * w2 + kf[t + 2] * w1 + kf[t + 3] * w0) * unpk(rb[t]);
            const u32 clr = cpeek(C_CLR);
            v4u c0 = v4u{nos(pk(r[7].x, r[6].x)), nos(pk(r[5].x, r[4].x)), nos(pk(r[3].x, r[2].x)), nos(pk(r[1].x, r[0].x))};
            v4u c1 = v4u{nos(pk(r[7].y, r[6].y)), nos(pk(r[5].y, r[4].y)), nos(pk(r[3].y, r[2].y)), nos(pk(r[1].y, r[0].y))};
            asm volatile("" : "+v"(c0), "+v"(c1));
            STAMP(1);
            if (!__builtin_amdgcn_readfirstlane(clr)) cwait(C_CLR);
            st128<SC1>(c0, rw, uo, 0);
            st128<SC1>(c1, rw, uo + 4608, 0);
            cadd(C_GO, 0x10u);
            STAMP(2);
            {
                const u32 vo = cp < 288 ? cp * 4 : OOB;
#pragma unroll
                for (int i = 0; i < 8; ++i) qq[i] = ld32<0>(rq, vo, (8 * b + i) * 1152);
                dvp = ld32<0>(mkrs(p.dv, D * 2), vo, 0);
            }
            *(LAS v4u*)L(L_ST + cp * 16) = c0;
            *(LAS v4u*)L(L_ST + 5120 + cp * 16) = c1;
            if (isConv && mw < 4) {
                LAS char* hb = L(L_H + mw * HCH);
#pragma unroll
            for (int i = 0; i < 4; ++i) {
                const v4u dd = hd[i];
                const u32 e0 = i < 3 ? __builtin_amdgcn_readfirstlane(hd[i < 3 ? i + 1 : 3].x) : 0u;
                const u32 e1 = i < 3 ? __builtin_amdgcn_readfirstlane(hd[i < 3 ? i + 1 : 3].y) : 0u;
                const u32 n0 = __builtin_amdgcn_update_dpp(e0, dd.x, 0x130, 0xF, 0xF, false);
                const u32 n1 = __builtin_amdgcn_update_dpp(e1, dd.y, 0x130, 0xF, 0xF, false);
                const u32 a01 = __builtin_amdgcn_alignbit(dd.y, dd.x, 16), a12 = __builtin_amdgcn_alignbit(dd.z, dd.y, 16),
                          a23 = __builtin_amdgcn_alignbit(dd.w, dd.z, 16), a3n = __builtin_amdgcn_alignbit(n0, dd.w, 16),
                          ann = __builtin_amdgcn_alignbit(n1, n0, 16);
                LAS char* o = hb + (lane + 64 * i + 2) * 16;
                *(LAS v4u*)(o) = dd;
                *(LAS v4u*)(o + HC) = v4u{a01, a12, a23, a3n};
                *(LAS v4u*)(o + 2 * HC) = v4u{dd.y, dd.z, dd.w, n0};
                *(LAS v4u*)(o + 3 * HC) = v4u{a12, a23, a3n, ann};
            }
            {
                const u32 h0 = __builtin_amdgcn_readfirstlane(hd[0].x), h1 = __builtin_amdgcn_readfirstlane(hd[0].y);
                if (lane < 8) {
                    const bool od = lane & 1;
                    const int sg = lane >> 1;
                    v4u z = {0u, 0u, 0u, 0u};
                    z.z = od && sg == 3 ? h0 << 16 : 0u;
                    z.w = !od || sg == 0 ? 0u : (sg == 1 ? h0 << 16 : (sg == 2 ? h0 : (h0 >> 16) | (h1 << 16)));
                    *(LAS v4u*)(hb + sg * HC + (lane & 1) * 16) = z;
                }
            }
                STAMP(7);
            }
            if (PF1 == 2 && isConv) {
                cwait_eq(C_GO, 0xFFu, 0x54u);
                const rsrc_t rg = mkrs(p.wg, D * F * 2), ru = mkrs(p.wu, D * F * 2);
#pragma unroll
                for (int i = 0; i < NP1; ++i) {
                    t1[2 * i] = ld128<0>(rg, pfo[i], 0);
                    t1[2 * i + 1] = ld128<0>(ru, pfo[i], 0);
                }
            }

            if (!isConv) {
                *(LAS v4u*)L(L_SQ + cp * 16) = v4u{qq[0], qq[1], qq[2], qq[3]};
                *(LAS v4u*)L(L_SQ + 5120 + cp * 16) = v4u{qq[4], qq[5], qq[6], qq[7]};
                *(LAS u32*)L(L_SQ + 10240 + cp * 4) = dvp;
            }
            if (isSel) {
                if (cp < 48) *(LAS v4u*)L(L_LK + cp * 16) = v4u{0u, 0u, 0u, 0u};
            }
        } else if (isConv) {
            LAS char* hb = L(L_H + lw * HCH);
            const int l = tid - 384;
            if (lane < 32) *(LAS v4u*)L(L_U + lw * UCH + lane * 16) = v4u{0u, 0u, 0u, 0u};
            f4 acc[8];
#pragma unroll
            for (int i = 0; i < 8; ++i) {
                acc[i] = f4{0.f, 0.f, 0.f, 0.f};
                asm volatile("" : "+v"(acc[i]));
            }
            u32 vo[4];
#pragma unroll
            for (int i = 0; i < 4; ++i) vo[i] = (64 * i + (l >> 2)) * 9216 + (l & 3) * 16;
            const int m1 = (lane & 15) + 1, kq = lane >> 4;
            u32 va = (u32)(uintptr_t)(hb + (m1 & 3) * HC + 16 * kq + 8 * (m1 >> 2));
            u32 vb = (u32)(uintptr_t)L(L_U + lw * UCH + (33 + 2 * (lane & 15) - kq) * 16 - 448);
            u32 uw = (u32)(uintptr_t)L(L_U + (l & 3) * UCH + (32 + (l >> 2)) * 16);
            u32 yw = (u32)(uintptr_t)L(L_Y + (32 * (kq & 1) + 2 * (lane & 15) + (kq >> 1)) * 64 + ((lw ^ lane) & 3) * 16);
            u32 yr = (u32)(uintptr_t)L(L_Y + (l >> 2) * 64 + ((l ^ (l >> 3)) & 3) * 16);
            u32 yg = W_Y + cj * 16384 + l * 16;
            asm volatile("" : "+v"(vo[0]), "+v"(vo[1]), "+v"(vo[2]), "+v"(vo[3]), "+v"(va), "+v"(vb), "+v"(uw), "+v"(yw), "+v"(yr), "+v"(yg));
            SB();
            STAMP(3);
            ln_part(rw, b, lw, lane, ra, rb, ts);
            asm volatile("" ::"v"(pf));
            cwait_eq(C_GO, 0xF0u, 0x50u);
            if (U_DELAY) __builtin_amdgcn_s_sleep(U_DELAY);
            cwait(C_F1);
            STAMP(4);
            v4u uv[4];
            poll_issue<4>(rw, vo, W_U + cj * 64, uv);
            poll_finish<4>(rw, vo, W_U + cj * 64, uv);
#pragma unroll
            for (int i = 0; i < 4; ++i) *(LAS v4u*)((LAS char*)(uintptr_t)uw + 1024 * i) = uv[i];
            STAMP(5);
            bar();
            STAMP(8);
            AF a0[4], a1[4];
            v4u b0[4], b1[4];
#pragma unroll
            for (int n = 0; n < 4; ++n) {
                DSR128(b0[n], vb, 448 + 512 * n);
                DSR64(a0[n].lo, va, 512 * n);
                DSR64(a0[n].hi, va, 512 * n + 8);
            }
            conv_phase<0>(va, vb, acc, a0, b0, a1, b1);
            conv_phase<1>(va, vb, acc, a1, b1, a0, b0);
            conv_phase<2>(va, vb, acc, a0, b0, a1, b1);
            conv_phase<3>(va, vb, acc, a1, b1, a0, b0);
            conv_phase<4>(va, vb, acc, a0, b0, a1, b1);
            conv_phase<5>(va, vb, acc, a1, b1, a0, b0);
            conv_phase<6>(va, vb, acc, a0, b0, a1, b1);
            conv_phase<7>(va, vb, acc, a1, b1, a0, b0);
            asm volatile("s_nop 7\n\ts_nop 1" : "+v"(acc[0]), "+v"(acc[1]), "+v"(acc[2]), "+v"(acc[3]), "+v"(acc[4]), "+v"(acc[5]), "+v"(acc[6]), "+v"(acc[7]));
            STAMP(6);
#pragma unroll
            for (int q = 0; q < 8; q += 2) {
                const u32 y0 = nos(pk(acc[q][0], acc[q][1])), y1 = nos(pk(acc[q][2], acc[q][3]));
                const u32 z0 = nos(pk(acc[q + 1][0], acc[q + 1][1])), z1 = nos(pk(acc[q + 1][2], acc[q + 1][3]));
                const auto s0 = __builtin_amdgcn_permlane16_swap(y0, z0, false, false);
                const auto s1 = __builtin_amdgcn_permlane16_swap(y1, z1, false, false);
                *(LAS v4u*)(uintptr_t)(yw + q * 2048) = v4u{s0[0], s1[0], s0[1], s1[1]};
            }
            cadd(C_YL);
            cwait<0>(C_YL, 5);
            {
                v4u yv[4];
#pragma unroll
                for (int i = 0; i < 4; ++i) yv[i] = *(LAS v4u*)(uintptr_t)(yr + 4096 * i);
#pragma unroll
                for (int i = 0; i < 4; ++i) st128<SC1>(yv[i], rw, yg + 4096 * i, 0);
            }
            cadd(C_YST);
            STAMP(7);
        } else {
            ln_part(rw, b, lw, lane, ra, rb, ts);
            asm volatile("" ::"v"(pf));
        }

        if (isMoe) {
            const int c = lane & 15, kq = lane >> 4;
            const GAS char* gp = (const GAS char*)(c < 8 ? p.wg : p.wu) + (size_t)(64 * mw + 8 * kq) * (F * 2) + mu * 128 + (c & 7) * 16;
            u32 xo = (u32)(uintptr_t)L(L_XS + (c & 7) * XSS + (64 * mw + 8 * kq) * 2);
            u32 ro = (u32)(uintptr_t)L(L_R + ((mw * 8 + 4 * (kq & 1)) * 128 + 8 * c) * 4);
            u32 po = W_PART + mu * 18432 + mw * 256 + (c & 7) * 2304 + (c >> 3) * 64 + kq * 16;
            u32 hwo = (u32)(uintptr_t)L(L_HA + mw * HAS + ((lane & 32) + 8 * ((lane & 15) >> 2) + 4 * ((lane >> 4) & 1) + (lane & 3)) * 2);
            u32 hro = (u32)(uintptr_t)L(L_HA + (c & 7) * HAS + 16 * kq), rro = (u32)(uintptr_t)L(L_R + (mw * 128 + lane) * 4);
            u32 ta[4];
#pragma unroll
            for (int i = 0; i < 4; ++i) {
                const int row = 16 * i + 4 * kq + (c >> 2);
                ta[i] = (u32)(uintptr_t)L(mw < 8 ? L_DN + row * DNS + 128 * mw + 8 * (c & 3) : L_DT + row * 128 + 8 * (c & 3));
                asm volatile("" : "+v"(ta[i]));
            }
            asm volatile("" : "+v"(xo), "+v"(ro), "+v"(po), "+v"(hwo), "+v"(hro), "+v"(rro));
            v4u gr[8], gf[2][8];
            v2u dl[2][4], dh[2][4];
            cwait<2>(C_WI);
            STAMP(11);
#pragma unroll
            for (int ks = 0; ks < 2; ++ks) {
#pragma unroll
                for (int e = 0; e < 8; ++e) gr[e] = *(const GAS v4u*)(gp + (size_t)(32 * ks + e) * (F * 2));
                SB();
                if (ks == 1) {
                    cwait<2>(C_DN);
#pragma unroll
                    for (int h = 0; h < 2; ++h)
#pragma unroll
                        for (int nt = 0; nt < 4; ++nt) {
                            asm volatile("ds_read_b64_tr_b16 %0, %1 offset:%2" : "=v"(dl[h][nt]) : "v"(ta[2 * h]), "i"(32 * nt));
                            asm volatile("ds_read_b64_tr_b16 %0, %1 offset:%2" : "=v"(dh[h][nt]) : "v"(ta[2 * h + 1]), "i"(32 * nt));
                        }
                    asm volatile("s_waitcnt lgkmcnt(0)" : "+v"(dl[0][0]), "+v"(dl[0][1]), "+v"(dl[0][2]), "+v"(dl[0][3]), "+v"(dl[1][0]), "+v"(dl[1][1]),
                                 "+v"(dl[1][2]), "+v"(dl[1][3]), "+v"(dh[0][0]), "+v"(dh[0][1]), "+v"(dh[0][2]), "+v"(dh[0][3]), "+v"(dh[1][0]),
                                 "+v"(dh[1][1]), "+v"(dh[1][2]), "+v"(dh[1][3]));
                    SB();
                }
#pragma unroll
                for (int nn = 0; nn < 8; ++nn)
#pragma unroll
                    for (int pp = 0; pp < 4; ++pp)
                        gf[ks][nn][pp] = __builtin_amdgcn_perm(gr[2 * pp + 1][nn >> 1], gr[2 * pp][nn >> 1], (nn & 1) ? 0x07060302u : 0x05040100u);
#pragma unroll
                for (int nn = 0; nn < 8; ++nn) asm volatile("" : "+v"(gf[ks][nn]));
                SB();
                if (ks == 0) STAMP(7);
            }
            STAMP(8);
            bar();
            {
                const v4u xa0 = *(LAS v4u*)(uintptr_t)xo, xa1 = *(LAS v4u*)(uintptr_t)(xo + 64);
                f4 g[8];
#pragma unroll
                for (int nn = 0; nn < 8; ++nn) g[nn] = mfma(xa1, gf[1][nn], mfma(xa0, gf[0][nn], f4{0.f, 0.f, 0.f, 0.f}));
#pragma unroll
                for (int r = 0; r < 4; ++r) {
                    const f4 o = kq < 2 ? f4{g[0][r], g[1][r], g[2][r], g[3][r]} : f4{g[4][r], g[5][r], g[6][r], g[7][r]};
                    *(LAS f4*)(uintptr_t)(ro + r * 512 + (kq >> 1) * 16) = o;
                }
            }
            bar();
            if (mw < 8) {
                float gv[9], uv[9];
#pragma unroll
                for (int w = 0; w < 9; ++w) {
                    gv[w] = *(LAS float*)(uintptr_t)(rro + w * 4096);
                    uv[w] = *(LAS float*)(uintptr_t)(rro + w * 4096 + 256);
                }
                SB();
                const float g = rbf(((gv[0] + gv[1]) + (gv[2] + gv[3])) + ((gv[4] + gv[5]) + (gv[6] + gv[7])) + gv[8]);
                const float u = rbf(((uv[0] + uv[1]) + (uv[2] + uv[3])) + ((uv[4] + uv[5]) + (uv[6] + uv[7])) + uv[8]);
                const float sg = rbf(g * __builtin_amdgcn_rcpf(1.0f + __expf(-g)));
                *(LAS u16*)(uintptr_t)hwo = (u16)pk(sg * u, 0.f);
            }
            bar();
            STAMP(9);
            {
                const v4u ha0 = *(LAS v4u*)(uintptr_t)hro, ha1 = *(LAS v4u*)(uintptr_t)(hro + 64);
                f4 y[4];
#pragma unroll
                for (int nt = 0; nt < 4; ++nt)
                    y[nt] = mfma(v4u{dl[1][nt].x, dl[1][nt].y, dh[1][nt].x, dh[1][nt].y}, ha1,
                                 mfma(v4u{dl[0][nt].x, dl[0][nt].y, dh[0][nt].x, dh[0][nt].y}, ha0, f4{0.f, 0.f, 0.f, 0.f}));
#pragma unroll
                for (int i = 0; i < 2; ++i) {
                    const f4 o = c < 8 ? y[2 * i] : y[2 * i + 1];
                    st128<SC1>(v4u{nos(asu(o[0])), nos(asu(o[1])), nos(asu(o[2])), nos(asu(o[3]))}, rw, po + 128 * i, 0);
                }
            }
            STAMP(10);
        } else if (isSel) {
            const int sw = mw < 4 ? mw : mw - 1;
            u32 vo[2] = {(u32)(sw * 64 + lane) * 16, (u32)(sw * 64 + lane) * 16 + 8192};
            v4u kv2[2];
            const int s0 = (64 * mw) / 72, s = cp / 72;
            u32 qo = (cp - 72 * s) * 16, xo = xs + 128 + cp * 16;
            asm volatile("" : "+v"(vo[0]), "+v"(vo[1]), "+v"(qo), "+v"(xo));
            if (!isK && K_DELAY) __builtin_amdgcn_s_sleep(K_DELAY);
            bar();
            if (mw != 4) {
                poll16<2>(rw, vo, W_KEY, kv2);
                u32 mx = max32(umax(umax(kv2[0].x, kv2[0].z), umax(kv2[1].x, kv2[1].z)));
                const auto s2 = __builtin_amdgcn_permlane32_swap(mx, mx, false, false);
                if (lane == 0) *(LAS u32*)L(L_WM + 4 * sw) = umax(s2[0], s2[1]);
            }
            STAMP(7);
            bar();
            if (mw != 4) {
                const v4u m0 = *(LAS v4u*)L(L_WM), m1 = *(LAS v4u*)L(L_WM + 16);
                const u32 thr = umin(umin(umin(m0.x, m0.y), umin(m0.z, m0.w)), umin(umin(m1.x, m1.y), umin(m1.z, m1.w)));
                u32 key[4] = {kv2[0].x, kv2[0].z, kv2[1].x, kv2[1].z};
                const u32 ms[4] = {kv2[0].y, kv2[0].w, kv2[1].y, kv2[1].w};
                u64 bm[4];
                u32 tot = 0;
#pragma unroll
                for (int i = 0; i < 4; ++i) {
                    bm[i] = __builtin_amdgcn_ballot_w64(key[i] >= thr);
                    tot += __builtin_popcountll(bm[i]);
                }
                if (tot <= 8u) {
                    u32 pos = 8 * sw;
#pragma unroll
                    for (int i = 0; i < 4; ++i) {
                        const u32 at = __builtin_amdgcn_mbcnt_hi((u32)(bm[i] >> 32), __builtin_amdgcn_mbcnt_lo((u32)bm[i], pos));
                        *(LAS v2u*)L(L_LK + (key[i] >= thr ? at * 8 : 1024u)) = v2u{key[i], ms[i]};
                        pos += __builtin_popcountll(bm[i]);
                    }
                } else {
                    NOUNROLL
                    for (int r = 0; r < 8; ++r) {
                        u32 mx = max32(umax(umax(key[0], key[1]), umax(key[2], key[3])));
                        const auto s2 = __builtin_amdgcn_permlane32_swap(mx, mx, false, false);
                        mx = umax(s2[0], s2[1]);
#pragma unroll
                        for (int i = 0; i < 4; ++i)
                            if (key[i] == mx) {
                                *(LAS v2u*)L(L_LK + (8 * sw + r) * 8) = v2u{key[i], ms[i]};
                                key[i] = 0u;
                            }
                    }
                }
            }
            bar();
            const v2u mine = *(LAS v2u*)L(L_LK + lane * 8);
            if (mw != 4) {
                u32 cnt = 0;
#pragma unroll
                for (int u = 0; u < 4; ++u) {
                    const v4u t = *(LAS v4u*)L(L_LK + sw * 64 + u * 16);
                    cnt += (t.x > mine.x) + (t.z > mine.x);
                }
                __hip_atomic_fetch_add((LAS u32*)L(L_RK + lane * 4), cnt, __ATOMIC_RELAXED, __HIP_MEMORY_SCOPE_WORKGROUP);
            }
            bar();
            STAMP(9);
            {
                const u32 rank = *(LAS u32*)L(L_RK + lane * 4);
                const int la = __builtin_ctzll(__builtin_amdgcn_ballot_w64(rank == (u32)s0) | (1ull << 63));
                const int lb = __builtin_ctzll(__builtin_amdgcn_ballot_w64(rank == (u32)s0 + 1u) | (1ull << 63));
                const u32 ka2 = __builtin_amdgcn_readlane(mine.x, la), kb2 = __builtin_amdgcn_readlane(mine.x, lb);
                const u32 ma2 = __builtin_amdgcn_readlane(mine.y, la), mb2 = __builtin_amdgcn_readlane(mine.y, lb);
                const v2u me = s == s0 ? v2u{ka2, ma2} : v2u{kb2, mb2};
                const v4u xr = ld128<0>(rq, ((2047 - me.x) & 2047) * 1152 + qo, 0);
                if (mw == 0) {
                    st64<SC1>(mine, rw, rank < 8u ? rank * 8 : OOB, xs);
                    if (rank < 8u) *(LAS v2u*)L(L_SEL + rank * 8) = mine;
                    LGKM0();
                    cset(C_XS);
                }
                const float mb = lo16(me.y), inv = __builtin_amdgcn_rcpf(hi16(me.y));
                v4u o;
#pragma unroll
                for (int i = 0; i < 4; ++i) o[i] = nos(pk2((unpk(xr[i]) - mb) * inv));
                st128<SC1>(o, rw, xo, 0);
            }
            STAMP(10);
        } else if (HELP_GU && !isConv) {
            const rsrc_t rg = mkrs(p.wg, D * F * 2), ru = mkrs(p.wu, D * F * 2);
            constexpr int NH = 576 - 72 * (NP1 + NP2), NI = (NH * 72 + 2303) / 2304;
            v4u t[2 * NI];
            cwait(C_GO, 0x54u);
#pragma unroll
            for (int i = 0; i < NI; ++i) {
                const u32 idx = ((xi - 28) * 9 + mw) * 64 + lane + 2304 * i, r = idx / 72, ch = idx - 72 * r;
                const u32 o = r < NH ? pf_off(72 * NP1 + r, ch, xq) : OOB;
                t[2 * i] = ld128<0>(rg, o, 0);
                t[2 * i + 1] = ld128<0>(ru, o, 0);
            }
            STAMP(7);
#pragma unroll
            for (int i = 0; i < 2 * NI; ++i) asm volatile("" ::"v"(t[i]));
            STAMP(9);
        }

        if (isK) {
            const u32 vo = cp < 288 ? cp * 4 : OOB;
            if (!isConv) {
                const v4u qa = *(LAS v4u*)L(L_SQ + cp * 16), qb = *(LAS v4u*)L(L_SQ + 5120 + cp * 16);
                qq[0] = qa.x; qq[1] = qa.y; qq[2] = qa.z; qq[3] = qa.w; qq[4] = qb.x; qq[5] = qb.y; qq[6] = qb.z; qq[7] = qb.w;
                dvp = *(LAS u32*)L(L_SQ + 10240 + cp * 4);
            }
            const f2 dv = unpk(dvp);
            const v4u kvc[2] = {*(LAS v4u*)L(L_ST + cp * 16), *(LAS v4u*)L(L_ST + 5120 + cp * 16)};
            cwait_eq(C_GO, 0xFu, 4u);
            f2 xn[8], pb[8];
#pragma unroll
            for (int t = 0; t < 8; ++t) {
                const f2 st = *(LAS f2*)L(L_STAT + t * 8);
                const f2 x = unpk(qq[t]);
                xn[t] = unpk(pk2((x - st.x) * st.y));
                const u32 c0 = kvc[0][(7 - t) >> 1], c1 = kvc[1][(7 - t) >> 1];
                const f2 kv = (t & 1) ? f2{lo16(c0), lo16(c1)} : f2{hi16(c0), hi16(c1)};
                pb[t] = x + xn[t] + xn[t] * (kv * dv);
            }
            u32 vy[2] = {(u32)((cp >> 2) * 16384 + (cp & 3) * 16), (u32)(((cp + 288) >> 2) * 16384 + (cp & 3) * 16)};
#pragma unroll
            for (int t = 0; t < 8; ++t) asm volatile("" : "+v"(xn[t]), "+v"(pb[t]));
            asm volatile("" : "+v"(vy[0]), "+v"(vy[1]));
            STAMP(11);
            if (isConv) {
                bar();
#pragma unroll
                for (int i = 0; i < 2 * NP1; ++i) asm volatile("" ::"v"(t1[i]));
                __builtin_amdgcn_s_sleep(4);
                if (HELP_DN) {
                    const rsrc_t rg = mkrs(p.wg, D * F * 2), ru = mkrs(p.wu, D * F * 2);
                    v4u t[2 * NP2];
#pragma unroll
                    for (int i = 0; i < NP2; ++i) {
                        const u32 o = cp < 288 ? pf_off(576 - 72 * NP2 + 4 * (18 * i + xi) + (u32)cp / 72, (u32)cp % 72, xq) : OOB;
                        t[2 * i] = ld128<0>(rg, o, 0);
                        t[2 * i + 1] = ld128<0>(ru, o, 0);
                    }
#pragma unroll
                    for (int i = 0; i < 2 * NP2; ++i) asm volatile("" ::"v"(t[i]));
                    STAMP(9);
                }
            }
            const u32 mask = cwait<8>(C_SEL);
            u32 sr[8];
#pragma unroll
            for (int t = 0; t < 8; ++t) sr[t] = ((mask >> t) & 1u) ? OOB : (8 * b + t) * 1152;
            cwait<8>(C_F2);
            STAMP(3);
            if (isConv) {
                cwait<4>(C_YST, 4);
                if (Y_DELAY) __builtin_amdgcn_s_sleep(Y_DELAY);
            } else {
                wait_word<4>(rw, W_Y + (18 * xq + 17) * 16384 + b * 64);
            }
            STAMP(4);
            if (cp < 288) {
                v4u yv[2];
                poll16<2>(rw, vy, W_Y + b * 64, yv);
                STAMP(5);
                const rsrc_t ro = mkrs(p.out, S * D * 2);
#pragma unroll
                for (int t = 0; t < 8; ++t) {
                    const u32 c0 = yv[0][t >> 1], c1 = yv[1][t >> 1];
                    const f2 y = (t & 1) ? f2{hi16(c0), hi16(c1)} : f2{lo16(c0), lo16(c1)};
                    st32<AUX_OUT>(pk2(pb[t] + xn[t] * y), ro, vo, sr[t]);
                }
                STAMP(6);
            }
        }
    }
    {
        u32 extra = 0;
        if (PROBE_COND) extra = 10 * (__builtin_amdgcn_readfirstlane(ts[PROBE_B]) - ts[PROBE_A]);
        if (extra > 30000) extra = 30000;
        while (__builtin_amdgcn_s_memrealtime() - pad0 < PADUS * 100 + extra) __builtin_amdgcn_s_sleep(1);
    }
}

extern "C" void kernel_launch(void* const* d_in, const int* in_sizes, int n_in, void* d_out, int out_size, void* d_ws, size_t ws_size,
                              hipStream_t stream) {
    Params p;
    p.q = (const u16*)d_in[0];
    p.k = (const u16*)d_in[1];
    p.v = (const u16*)d_in[2];
    p.sk = (const u16*)d_in[3];
    p.h = (const u16*)d_in[4];
    p.dv = (const u16*)d_in[5];
    p.wr = (const u16*)d_in[7];
    p.wg = (const u16*)d_in[8];
    p.wu = (const u16*)d_in[9];
    p.wd = (const u16*)d_in[10];
    p.out = (u16*)d_out;
    p.ws = (char*)d_ws + (WS_AT_END ? ((ws_size - WS_BYTES) & ~(size_t)4095) : 0);
    fused_kernel<<<256, NT, 0, stream>>>(p);
}
```

```cpp
#define W0_EARLY 0
#define UB2 3
#include <hip/hip_runtime.h>
#include <stdint.h>

typedef unsigned short u16;
typedef unsigned u32;
typedef unsigned long long u64;
typedef u32 v4u __attribute__((ext_vector_type(4)));
typedef u32 v2u __attribute__((ext_vector_type(2)));
typedef float f4 __attribute__((ext_vector_type(4)));
typedef float f2 __attribute__((ext_vector_type(2)));
typedef __bf16 bf8 __attribute__((ext_vector_type(8)));
typedef __bf16 bf2 __attribute__((ext_vector_type(2)));
#define GAS __attribute__((address_space(1)))
#define LAS __attribute__((address_space(3)))
typedef __amdgpu_buffer_rsrc_t rsrc_t;

#define AUX_IN 0
#define CLR_AUX 16
#define AUX_OUT 16
#define WS_AT_END 1
#define H_DELAY 0
#define Q_DELAY 0
#define U_DELAY 6
#define F_DELAY 36
#define K_DELAY 8
#define Y_DELAY 8
#define YDIRECT 0
#define X_DELAY 6
#define CODE_KB 28
#define HELP_C3 1
#define PF1 2
#define SPRIO 1
#define NP1 0
#define NP2 2
#define TR_EARLY 0
#define DMA_AT 1
#define TW0C 0
#define TW0M 4
#define TB2 4
#define G1_DELAY 0
#define T2_DELAY 0
#define FS_DELAY 24
#ifndef M_HINT
#define M_HINT 0
#endif
#define ALIAS 0
#define X_HINT 1
#define PH_AT 2
#define P_DELAY 0
#define WI_AT 64
#define W_DELAY 0
#define U0M 3
#define U0H 4
#define HELP_GU 1
#define HELP_DN 1

constexpr int S = 2048, D = 576, F = 4608;
constexpr int NT = 704;
constexpr u32 SENT = 0xFFFFFFFFu;
constexpr int SC1 = 16;
constexpr u32 SPIN_CAP = 1u << 16;
constexpr u32 OOB = 0x40000000u;

constexpr int XSB = 9344, US = 9216;
constexpr int W_E = 0, W_F3 = 1024, W_F1 = 4096, W_F2 = W_F1 + 2048, W_KEY = W_F2 + 2048, W_XS = W_KEY + 256 * 64, W_U = W_XS + 8 * XSB,
              W_Y = ALIAS ? W_U : W_U + 256 * US, W_PART = W_U + 256 * US + 144 * 16384, W_END = W_PART + 72 * 18432, W_DBG = W_END, W_PH = W_END + 524288;
constexpr int WS_BYTES = W_END + (1 << 20);
static_assert(W_U % 128 == 0 && XSB % 128 == 0, "align");
constexpr int L_C = 0, L_STAT = L_C + 64, L_SEL = L_STAT + 64, L_WM = L_SEL + 64, L_RED = L_WM + 64, L_ST = 512, L_RA = L_ST + 320 * 96;
constexpr int HC = 4160, HCH = 4 * HC, L_H = L_RA, UCH = 290 * 16, L_U = L_H + 4 * HCH, L_Y = L_U + 4 * UCH;
constexpr int L_R = L_RA, L_HA = L_R + 9 * 8 * 128 * 4, HAS = 144, DNS = 1056, L_DN = L_HA + 1280, L_DT = L_DN + 64 * DNS, L_END = L_DT + 8192;
constexpr int L_LK = L_RA, L_RK = L_LK + 512;
static_assert(L_Y + 16384 <= L_END && L_RA % 256 == 0 && L_DN % 256 == 0, "lds");
enum { C_CLR = 0, C_F1 = 1, C_F2 = 2, C_SEL = 3, C_UST = 4, C_CNT = 5, C_ST = 6, C_YST = 7, C_WI = 8, C_GO = 9, C_XS = 10, C_YL = 11, C_DN = 12, C_W0 = 13, C_TAG = 14 };

struct Params {
    const u16 *q, *k, *v, *sk, *h, *dv, *wr, *wg, *wu, *wd;
    u16* out;
    char* ws;
};

__device__ __forceinline__ float asf(u32 x) { return __builtin_bit_cast(float, x); }
__device__ __forceinline__ u32 asu(float x) { return __builtin_bit_cast(u32, x); }
__device__ __forceinline__ float lo16(u32 x) { return asf(x << 16); }
__device__ __forceinline__ float hi16(u32 x) { return asf(x & 0xFFFF0000u); }
__device__ __forceinline__ f2 unpk(u32 x) { return f2{lo16(x), hi16(x)}; }
__device__ __forceinline__ u32 pk(float a, float b) { return __builtin_bit_cast(u32, __builtin_convertvector(f2{a, b}, bf2)); }
__device__ __forceinline__ u32 pk2(f2 a) { return pk(a.x, a.y); }
__device__ __forceinline__ float rbf(float a) { return lo16(pk(a, a)); }
__device__ __forceinline__ u32 nos(u32 x) { return x < 0xFFFFFFFEu ? x : 0xFFFFFFFEu; }
__device__ __forceinline__ u32 umax(u32 a, u32 b) { return a > b ? a : b; }
__device__ __forceinline__ u32 umin(u32 a, u32 b) { return a < b ? a : b; }
__device__ __forceinline__ bool valid4(v4u v) { return umax(umax(v.x, v.y), umax(v.z, v.w)) != SENT; }
__device__ __forceinline__ rsrc_t mkrs(const void* p, u32 bytes) { return __builtin_amdgcn_make_buffer_rsrc((void*)p, 0, bytes, 0x00020000); }
template <int AUX> __device__ __forceinline__ u32 ld32(rsrc_t r, u32 vo, u32 so) { return __builtin_amdgcn_raw_buffer_load_b32(r, vo, so, AUX); }
template <int AUX> __device__ __forceinline__ v2u ld64(rsrc_t r, u32 vo, u32 so) { return __builtin_amdgcn_raw_buffer_load_b64(r, vo, so, AUX); }
template <int AUX> __device__ __forceinline__ v4u ld128(rsrc_t r, u32 vo, u32 so) { return __builtin_amdgcn_raw_buffer_load_b128(r, vo, so, AUX); }
template <int AUX> __device__ __forceinline__ void st32(u32 v, rsrc_t r, u32 vo, u32 so) { __builtin_amdgcn_raw_buffer_store_b32(v, r, vo, so, AUX); }
template <int AUX> __device__ __forceinline__ void st64(v2u v, rsrc_t r, u32 vo, u32 so) { __builtin_amdgcn_raw_buffer_store_b64(v, r, vo, so, AUX); }
template <int AUX> __device__ __forceinline__ void st128(v4u v, rsrc_t r, u32 vo, u32 so) { __builtin_amdgcn_raw_buffer_store_b128(v, r, vo + so, 0, AUX); }
#define FENCE() asm volatile("" ::: "memory")
#define DRAIN() asm volatile("s_waitcnt vmcnt(0)" ::: "memory")
#define LGKM0() asm volatile("s_waitcnt lgkmcnt(0)" ::: "memory")
#define SB() __builtin_amdgcn_sched_barrier(0)
#define NOUNROLL _Pragma("unroll 1")
__device__ __forceinline__ void bar() {
    SB();
    LGKM0();
    __builtin_amdgcn_s_barrier();
    FENCE();
    SB();
}
template <int CTRL> __device__ __forceinline__ u32 dpp(u32 x) { return (u32)__builtin_amdgcn_update_dpp(0, (int)x, CTRL, 0xF, 0xF, true); }
__device__ __forceinline__ float sum32(float x) {
    x += asf(dpp<0xB1>(asu(x)));
    x += asf(dpp<0x4E>(asu(x)));
    x += asf(dpp<0x141>(asu(x)));
    x += asf(dpp<0x140>(asu(x)));
    auto r = __builtin_amdgcn_permlane16_swap(asu(x), asu(x), false, false);
    return asf(r[0]) + asf(r[1]);
}
__device__ __forceinline__ u32 max32(u32 x) {
    x = umax(x, dpp<0xB1>(x));
    x = umax(x, dpp<0x4E>(x));
    x = umax(x, dpp<0x141>(x));
    x = umax(x, dpp<0x140>(x));
    auto r = __builtin_amdgcn_permlane16_swap(x, x, false, false);
    return umax(r[0], r[1]);
}
__device__ __forceinline__ f4 mfma(v4u a, v4u b, f4 c) {
    return __builtin_amdgcn_mfma_f32_16x16x32_bf16(__builtin_bit_cast(bf8, a), __builtin_bit_cast(bf8, b), c, 0, 0, 0);
}

#define STAMP(i) do { } while (0)

__shared__ __attribute__((aligned(16))) char lds_raw[L_END];

__device__ __forceinline__ LAS char* L(int off) { return (LAS char*)lds_raw + off; }
__device__ __forceinline__ void cset(int w, u32 v = 1u) { FENCE(); *(volatile LAS u32*)L(L_C + 4 * w) = v; }
template <int SL = 1> __device__ __forceinline__ u32 cwait(int w, u32 want = 1u) {
    u32 x = 0;
    NOUNROLL
    for (u32 n = 0; n < (1u << 22); ++n) {
        x = __builtin_amdgcn_readfirstlane(*(volatile LAS u32*)L(L_C + 4 * w));
        if (x >= want) break;
        if (SL) __builtin_amdgcn_s_sleep(SL);
    }
    FENCE();
    return x;
}
__device__ __forceinline__ u32 cpeek(int w) { return *(volatile LAS u32*)L(L_C + 4 * w); }
__device__ __forceinline__ void cadd(int w, u32 v = 1u) {
    u64 ex;
    asm volatile("s_mov_b64 %0, exec\n\ts_mov_b64 exec, 1\n\tds_add_u32 %1, %2\n\ts_mov_b64 exec, %0"
                 : "=&s"(ex) : "v"((u32)(uintptr_t)L(L_C + 4 * w)), "v"(v) : "memory");
}
template <int SL = 1> __device__ __forceinline__ void cwait_eq(int w, u32 mask, u32 want) {
    NOUNROLL
    for (u32 n = 0; n < (1u << 22); ++n) {
        if ((__builtin_amdgcn_readfirstlane(*(volatile LAS u32*)L(L_C + 4 * w)) & mask) == want) break;
        if (SL) __builtin_amdgcn_s_sleep(SL);
    }
    FENCE();
}
__device__ __forceinline__ void wait_flags(rsrc_t rw, int base, u32 tlo, u32 thi, int lane) {
    NOUNROLL
    for (u32 n = 0; n < SPIN_CAP; ++n) {
        FENCE();
        const v4u f0 = ld128<SC1>(rw, lane * 16, base), f1 = ld128<SC1>(rw, lane * 16 + 1024, base);
        if (__all((((f0.x ^ tlo) | (f0.z ^ tlo) | (f1.x ^ tlo) | (f1.z ^ tlo)) | ((f0.y ^ thi) | (f0.w ^ thi) | (f1.y ^ thi) | (f1.w ^ thi))) == 0u)) break;
        __builtin_amdgcn_s_sleep(8);
    }
    FENCE();
}
__device__ __forceinline__ void wait_flag(rsrc_t rw, int off, u32 tlo, u32 thi) {
    NOUNROLL
    for (u32 n = 0; n < SPIN_CAP; ++n) {
        FENCE();
        const v2u f = ld64<SC1>(rw, 0, off);
        if (f.x == tlo && f.y == thi) break;
        __builtin_amdgcn_s_sleep(2);
    }
    FENCE();
}
template <int SL> __device__ __forceinline__ void wait_word(rsrc_t rw, u32 off) {
    NOUNROLL
    for (u32 n = 0; n < SPIN_CAP; ++n) {
        FENCE();
        if (ld32<SC1>(rw, 0, off) != SENT) break;
        __builtin_amdgcn_s_sleep(SL);
    }
    FENCE();
}
template <int N> __device__ __forceinline__ void poll_issue(rsrc_t rw, const u32 (&vo)[N], u32 so, v4u (&v)[N]) {
    FENCE();
#pragma unroll
    for (int i = 0; i < N; ++i) v[i] = ld128<SC1>(rw, vo[i], so);
    SB();
}
template <int N> __device__ __forceinline__ void poll_finish(rsrc_t rw, const u32 (&vo)[N], u32 so, v4u (&v)[N]) {
    NOUNROLL
    for (u32 n = 0; n < SPIN_CAP; ++n) {
        bool ok[N], all = true;
#pragma unroll
        for (int i = 0; i < N; ++i) { ok[i] = valid4(v[i]); all &= ok[i]; }
        if (__all(all)) break;
        FENCE();
#pragma unroll
        for (int i = 0; i < N; ++i) if (!ok[i]) v[i] = ld128<SC1>(rw, vo[i], so);
    }
    FENCE();
}
template <int N> __device__ __forceinline__ void poll16(rsrc_t rw, const u32 (&vo)[N], u32 so, v4u (&v)[N]) {
    poll_issue<N>(rw, vo, so, v);
    poll_finish<N>(rw, vo, so, v);
}
template <int N, int SL> __device__ __forceinline__ void poll16s(rsrc_t rw, const u32 (&vo)[N], u32 so, v4u (&v)[N]) {
    NOUNROLL
    for (u32 n = 0; n < SPIN_CAP; ++n) {
        FENCE();
        bool all = true;
#pragma unroll
        for (int i = 0; i < N; ++i) v[i] = ld128<SC1>(rw, vo[i], so);
#pragma unroll
        for (int i = 0; i < N; ++i) all &= valid4(v[i]);
        if (__all(all)) break;
        __builtin_amdgcn_s_sleep(SL);
    }
    FENCE();
}
__device__ __forceinline__ u32 selmask(int b) {
    const int lane = threadIdx.x & 63;
    const u32 rel = (2047u - (*(LAS u32*)L(L_SEL + (lane & 7) * 8) & 0xFFFFu)) - 8u * b;
    u32 m = rel < 8u ? 1u << rel : 0u;
    m |= dpp<0xB1>(m);
    m |= dpp<0x4E>(m);
    m |= dpp<0x141>(m);
    return m;
}

#define DSR64(dst, addr, off) asm volatile("ds_read_b64 %0, %1 offset:%2" : "=v"(dst) : "v"(addr), "i"(off))
#define DSR128(dst, addr, off) asm volatile("ds_read_b128 %0, %1 offset:%2" : "=v"(dst) : "v"(addr), "i"(off))
#define MFMA_IP(acc, a, b) asm volatile("v_mfma_f32_16x16x32_bf16 %0, %1, %2, %0" : "+v"(acc) : "v"(a), "v"(b))

struct AF { v2u lo, hi; };
__device__ __forceinline__ v4u cat(const AF& a) { return v4u{a.lo.x, a.lo.y, a.hi.x, a.hi.y}; }

template <int I0> __device__ __forceinline__ void conv_phase(u32 va, u32 vb, f4 (&acc)[8], AF (&alo)[4], v4u (&blo)[4], AF (&alo2)[4], v4u (&blo2)[4]) {
    AF ahi[4];
    v4u bhi[4];
    constexpr int OA = 64 * I0, OB = 448 - 64 * I0;
    asm volatile("s_waitcnt lgkmcnt(0)" : "+v"(alo[0].lo), "+v"(alo[0].hi), "+v"(alo[1].lo), "+v"(alo[1].hi), "+v"(alo[2].lo), "+v"(alo[2].hi), "+v"(alo[3].lo),
                 "+v"(alo[3].hi), "+v"(blo[0]), "+v"(blo[1]), "+v"(blo[2]), "+v"(blo[3]));
    SB();
#pragma unroll
    for (int n = 0; n < 16; ++n) {
        const int e = n >> 2, b = n & 3;
        MFMA_IP(acc[e + b], cat(alo[e]), blo[b]);
        if (n < 4) DSR128(bhi[n], vb, OB + 512 * (4 + n));
        else if (n < 12) {
            if (n & 1) DSR64(ahi[(n - 4) >> 1].hi, va, OA + 512 * (4 + ((n - 4) >> 1)) + 8);
            else DSR64(ahi[(n - 4) >> 1].lo, va, OA + 512 * (4 + ((n - 4) >> 1)));
        }
        SB();
    }
    asm volatile("s_waitcnt lgkmcnt(0)" : "+v"(ahi[0].lo), "+v"(ahi[0].hi), "+v"(ahi[1].lo), "+v"(ahi[1].hi), "+v"(ahi[2].lo), "+v"(ahi[2].hi), "+v"(ahi[3].lo),
                 "+v"(ahi[3].hi), "+v"(bhi[0]), "+v"(bhi[1]), "+v"(bhi[2]), "+v"(bhi[3]));
    SB();
    int n = 0;
#pragma unroll
    for (int half = 0; half < 2; ++half)
#pragma unroll
        for (int e = 0; e < 4; ++e)
#pragma unroll
            for (int b = 0; b < 4 - e; ++b) {
                if (half == 0) MFMA_IP(acc[4 + e + b], cat(alo[e]), bhi[b]);
                else MFMA_IP(acc[4 + e + b], cat(ahi[e]), blo[b]);
                if (I0 < 7) {
                    if (n < 4) DSR128(blo2[n], vb, OB - 64 + 512 * n);
                    else if (n < 12) {
                        if (n & 1) DSR64(alo2[(n - 4) >> 1].hi, va, OA + 64 + 512 * ((n - 4) >> 1) + 8);
                        else DSR64(alo2[(n - 4) >> 1].lo, va, OA + 64 + 512 * ((n - 4) >> 1));
                    }
                }
                ++n;
                SB();
            }
}

__device__ __forceinline__ void touch_unit(rsrc_t rg, rsrc_t ru, u32 vt, u32 u, int xq, u32& a, u32& b) {
    const u32 g = u / 9u, mw = u - 9u * g;
    const u32 so = (64u * mw + 32u * (g >> 3) + (g & 7u)) * (F * 2) + xq * 1152;
    a = ld32<0>(rg, vt, so);
    b = ld32<0>(ru, vt, so);
}

template <class TS>
__device__ __forceinline__ void ln_part(rsrc_t rw, int b, int lw, int lane, const u32 (&xq9)[11], const u32 (&wr9)[9], TS& ts) {
    const int hf = lane >> 5, l32 = lane & 31, tl = 2 * lw + hf, tok = 8 * b + tl;
    u32 ko = l32 == 0 ? W_KEY + b * 64 + tl * 8 : OOB;
    asm volatile("" : "+v"(ko));
    asm volatile("" ::"v"(xq9[8]));
    STAMP(10);
    f2 x[9];
    f2 s2 = {0.f, 0.f};
#pragma unroll
    for (int i = 0; i < 9; ++i) { x[i] = unpk(xq9[i]); s2 += x[i]; }
    const float mean = sum32(s2.x + s2.y) * (1.0f / D);
    f2 v2 = {0.f, 0.f};
#pragma unroll
    for (int i = 0; i < 9; ++i) { const f2 dl = x[i] - mean; v2 += dl * dl; }
    const u32 clr = cpeek(C_CLR);
    const float var = rbf(sum32(v2.x + v2.y) * (1.0f / D));
    const float mb = rbf(mean), sd = rbf(__builtin_amdgcn_sqrtf(rbf(var + 1e-5f)));
    const float inv = __builtin_amdgcn_rcpf(sd);
    float a0 = 0.f, a1 = 0.f;
#pragma unroll
    for (int i = 0; i < 9; ++i) {
        const u32 xn = pk2((x[i] - mb) * inv);
        if (i & 1) a1 = __builtin_amdgcn_fdot2_f32_bf16(__builtin_bit_cast(bf2, xn), __builtin_bit_cast(bf2, wr9[i]), a1, false);
        else a0 = __builtin_amdgcn_fdot2_f32_bf16(__builtin_bit_cast(bf2, xn), __builtin_bit_cast(bf2, wr9[i]), a0, false);
    }
    const float rs = sum32(a0 + a1);
    const u32 rb = pk(rs, rs) & 0xFFFFu;
    u32 ord = (rb & 0x8000u) ? (~rb & 0xFFFFu) : (rb | 0x8000u);
    if ((rb & 0x7FFFu) > 0x7F80u) ord = 0u;
    v2u rec = {(ord << 16) | (u32)(2047 - tok), nos(pk(mb, sd))};
    asm volatile("" : "+v"(rec));
    STAMP(1);
    if (!__builtin_amdgcn_readfirstlane(clr)) cwait(C_CLR);
    st64<SC1>(rec, rw, ko, 0);
    if (l32 == 0) *(LAS f2*)L(L_STAT + tl * 8) = f2{mb, inv};
    cadd(C_GO);
    STAMP(2);
}

typedef u32 v16u __attribute__((ext_vector_type(16)));
typedef u32 v8u __attribute__((ext_vector_type(8)));
__device__ __forceinline__ u64 mk64(u32 lo, u32 hi) { return ((u64)hi << 32) | lo; }

__global__ void __launch_bounds__(NT, 3) fused_kernel(Params) {
    v16u ka;
    v8u kb;
    asm volatile("s_load_dwordx16 %0, %2, 0x0\n\ts_load_dwordx8 %1, %2, 0x40" : "=&s"(ka), "=&s"(kb) : "s"(__builtin_amdgcn_kernarg_segment_ptr()));
    const int tid = threadIdx.x, lane = tid & 63, wave = __builtin_amdgcn_readfirstlane(tid >> 6);
    const int b = 32 * (blockIdx.x & 7) + (blockIdx.x >> 3);
    u32 ts[12];
    u64 pc;
    asm volatile("s_getpc_b64 %0" : "=s"(pc));
    STAMP(0);
    const bool isK = (u32)(wave - 1) < 5u, isL = (u32)(wave - 6) < 4u;
    const int cp = tid - 64;
    const int lw = wave - 6;
    const int mw = wave - 1;
    u32 s1 = b * 9216, va0 = (u32)cp < 288u ? cp * 4 : OOB;
    asm volatile("" : "+s"(s1), "+v"(va0));
    u32 pf = 0;
    if (wave == 0) {
        if (CODE_KB && blockIdx.x < 32) pf = *(const GAS u32*)((pc & ~255ull) + umin((blockIdx.x >> 3) * 64 + lane, CODE_KB * 8 - 1) * 128);
        if (lane < 16) *(LAS u32*)L(L_C + 4 * lane) = 0u;
    }
    SB();
    asm volatile("s_waitcnt lgkmcnt(0)" : "+s"(ka), "+s"(kb)::"memory");
    Params p;
    p.q = (const u16*)mk64(ka[0], ka[1]);
    p.k = (const u16*)mk64(ka[2], ka[3]);
    p.v = (const u16*)mk64(ka[4], ka[5]);
    p.sk = (const u16*)mk64(ka[6], ka[7]);
    p.h = (const u16*)mk64(ka[8], ka[9]);
    p.dv = (const u16*)mk64(ka[10], ka[11]);
    p.wr = (const u16*)mk64(ka[12], ka[13]);
    p.wg = (const u16*)mk64(ka[14], ka[15]);
    p.wu = (const u16*)mk64(kb[0], kb[1]);
    p.wd = (const u16*)mk64(kb[2], kb[3]);
    p.out = (u16*)mk64(kb[4], kb[5]);
    p.ws = (char*)mk64(kb[6], kb[7]);
    const rsrc_t rw = mkrs(p.ws, WS_BYTES);
    const rsrc_t rq = mkrs(p.q, S * D * 2);

    u32 ra[11], rb[9];
    v4u sk;
    if (isK) {
        const rsrc_t rk = mkrs(p.k, S * D * 2), rv = mkrs(p.v, S * D * 2);
        sk = ld128<0>(mkrs(p.sk, D * 8), cp * 16, 0);
#pragma unroll
        for (int i = 0; i < 3; ++i) ra[i] = ld32<AUX_IN>(rk, va0 + 1152 * i, s1 - 3456);
#pragma unroll
        for (int i = 0; i < 8; ++i) {
            ra[3 + i] = ld32<AUX_IN>(rk, va0 + 1152 * (i & 3), s1 + 4608 * (i >> 2));
            rb[i] = ld32<AUX_IN>(rv, va0 + 1152 * (i & 3), s1 + 4608 * (i >> 2));
        }
    } else if (isL) {
        const rsrc_t rr = mkrs(p.wr, D * 2);
        const u32 vq = (2 * lw + (lane >> 5)) * 1152 + (lane & 31) * 4;
        if (Q_DELAY) __builtin_amdgcn_s_sleep(Q_DELAY);
#pragma unroll
        for (int i = 0; i < 9; ++i) ra[i] = ld32<AUX_IN>(rq, vq + 128 * i, s1);
#pragma unroll
        for (int i = 0; i < 9; ++i) rb[i] = ld32<0>(rr, (lane & 31) * 4 + 128 * i, 0);
    }
    STAMP(11);
    bar();
    int wg = blockIdx.x;
    asm volatile("" : "+s"(wg));
    const int xq = wg & 7, xi = wg >> 3;
    const bool isConv = xi < 18, isMoe = xi >= 18 && xi < 27, isSel = xi == 27;
    const int cj = 18 * xq + xi, mu = 9 * xq + xi - 18;
    const u32 xs = W_XS + xq * XSB;
    const v4u s4 = {SENT, SENT, SENT, SENT};

    if (wave == 10) {
        if (isSel) {
            cwait<2>(C_CLR);
            const v2u tag = *(LAS v2u*)L(L_C + 4 * C_TAG);
            st128<SC1>(s4, rw, lane < 4 ? xs + lane * 16 : OOB, 0);
            DRAIN();
            if (lane == 0) st64<SC1>(tag, rw, 0, W_F3 + xq * 128);
            return;
        }
        constexpr int A1 = 9 * U0M, A2 = 144 - 18 * UB2, NHI = (A2 - A1 + 3) / 4;
        const rsrc_t rg = mkrs(p.wg, D * F * 2), ru = mkrs(p.wu, D * F * 2);
        const u32 vt = (lane & 15) < 9 ? (lane >> 4) * (8 * F * 2) + (lane & 15) * 128 : OOB;
        u32 tv[2 * (NHI > UB2 ? NHI : UB2)];
        if (isConv) {
            bar();
            cwait(C_W0);
#pragma unroll
            for (int i = 0; i < UB2; ++i) touch_unit(rg, ru, vt, A2 + 18 * i + xi, xq, tv[2 * i], tv[2 * i + 1]);
#pragma unroll
            for (int i = 0; i < 2 * UB2; ++i) asm volatile("" ::"v"(tv[i]));
            return;
        }
        if (W0_EARLY) cwait(C_UST);
        else cwait_eq(C_GO, 0xF0u, 0x50u);
        if (W_DELAY) __builtin_amdgcn_s_sleep(W_DELAY);
        STAMP(1);
        if (!isMoe) {
#pragma unroll
            for (int i = 0; i < NHI; ++i) {
                if (i == U0H) cwait(C_GO, 0x54u);
                touch_unit(rg, ru, vt, umin(A1 + 4 * i + xi - 28, A2 - 1), xq, tv[2 * i], tv[2 * i + 1]);
            }
#pragma unroll
            for (int i = 0; i < 2 * NHI; ++i) asm volatile("" ::"v"(tv[i]));
            return;
        }
#pragma unroll
        for (int i = 0; i < U0M; ++i) touch_unit(rg, ru, vt, 9 * i + xi - 18, xq, tv[2 * i], tv[2 * i + 1]);
        cwait(C_GO, 0x54u);
        STAMP(2);
        {
            const v4u sd = {(u32)(u64)p.wd, (u32)((u64)p.wd >> 32) & 0xFFFFu, (u32)(F * D * 2), 0x00020000u};
            u32 ro = 64 * mu * 1152;
            asm volatile("s_mov_b32 m0, %2\n\ts_nop 0\n\t.rept %c4\n\tbuffer_load_dwordx4 %1, %3, %0 offen lds\n\ts_add_u32 m0, m0, 1056\n\t"
                         "s_add_u32 %0, %0, 1152\n\t.endr"
                         : "+s"(ro) : "v"(lane * 16), "s"((u32)(uintptr_t)L(L_DN)), "s"(sd), "i"(WI_AT) : "memory", "scc");
            if (WI_AT < 64) {
                cset(C_WI);
                asm volatile("s_mov_b32 m0, %2\n\ts_nop 0\n\t.rept %c4\n\tbuffer_load_dwordx4 %1, %3, %0 offen lds\n\ts_add_u32 m0, m0, 1056\n\t"
                             "s_add_u32 %0, %0, 1152\n\t.endr"
                             : "+s"(ro) : "v"(lane * 16), "s"((u32)(uintptr_t)L(L_DN + WI_AT * DNS)), "s"(sd), "i"(64 - WI_AT) : "memory", "scc");
            }
            ro = 64 * mu * 1152;
            asm volatile("s_mov_b32 m0, %2\n\ts_nop 0\n\t.rept 8\n\tbuffer_load_dwordx4 %1, %3, %0 offen lds\n\ts_add_u32 m0, m0, 1024\n\t"
                         "s_add_u32 %0, %0, 9216\n\t.endr"
                         : "+s"(ro) : "v"((lane >> 3) * 1152 + 1024 + (lane & 7) * 16), "s"((u32)(uintptr_t)L(L_DT)), "s"(sd) : "memory", "scc");
        }
        cset(C_WI);
        STAMP(3);
        bar();
        DRAIN();
#pragma unroll
        for (int i = 0; i < 2 * U0M; ++i) asm volatile("" ::"v"(tv[i]));
        STAMP(4);
        bar();
    } else if (wave == 0) {
        const v2u e = ld64<SC1>(rw, 0, W_E);
#pragma unroll
        for (int i = 0; i < 9; ++i) st128<SC1>(s4, rw, lane * 16 + 1024 * i, W_U + b * US);
        st128<SC1>(s4, rw, lane < 4 ? W_KEY + b * 64 + lane * 16 : OOB, 0);
        cset(C_UST);
        STAMP(10);
        DRAIN();
        asm volatile("" ::"v"(pf));
        const u64 tag = (((u64)e.y << 32) | e.x) + 1ull;
        const u32 tlo = __builtin_amdgcn_readfirstlane((u32)tag), thi = __builtin_amdgcn_readfirstlane((u32)(tag >> 32));
        if (lane == 0) st64<SC1>(v2u{tlo, thi}, rw, 0, W_F1 + wg * 8);
        cset(C_TAG, tlo);
        cset(C_TAG + 1, thi);
        cset(C_CLR);
        STAMP(1);
        if (isMoe) {
#pragma unroll
            for (int i = 0; i < 18; ++i) st128<SC1>(s4, rw, lane * 16 + 1024 * i, W_PART + mu * 18432);
            DRAIN();
            if (lane == 0) st64<SC1>(v2u{tlo, thi}, rw, 0, W_F2 + wg * 8);
            wait_flag(rw, W_F3 + xq * 128, tlo, thi);
            __builtin_amdgcn_s_setprio(3);
            STAMP(4);
            {
                u32 vo[1] = {(u32)(lane & 3) * 16};
                v4u sv[1];
                poll16s<1, 2>(rw, vo, xs, sv);
                if (lane < 4) *(LAS v4u*)L(L_SEL + lane * 16) = sv[0];
                cset(C_XS);
                LGKM0();
            }
            STAMP(6);
            cset(C_SEL, 0x100u | selmask(b));
            bar();
            const v4u f0 = ld128<SC1>(rw, lane * 16, W_F2), f1 = ld128<SC1>(rw, lane * 16 + 1024, W_F2);
            bar();
            if (!__all((((f0.x ^ tlo) | (f0.z ^ tlo) | (f1.x ^ tlo) | (f1.z ^ tlo)) | ((f0.y ^ thi) | (f0.w ^ thi) | (f1.y ^ thi) | (f1.w ^ thi))) == 0u))
                wait_flags(rw, W_F2, tlo, thi, lane);
            cset(C_F2);
        } else if (isSel) {
            if (lane == 0) st64<SC1>(v2u{tlo, thi}, rw, 0, W_F2 + wg * 8);
            __builtin_amdgcn_s_sleep(FS_DELAY);
            wait_flags(rw, W_F1, tlo, thi, lane);
            STAMP(2);
            bar();
            if (xq == 0 && lane == 0) st64<SC1>(v2u{tlo, thi}, rw, 0, W_E);
            bar();
            bar();
            bar();
            cwait(C_XS);
            cset(C_SEL, 0x100u | selmask(b));
            __builtin_amdgcn_s_sleep(32);
            wait_flags(rw, W_F2, tlo, thi, lane);
            cset(C_F2);
        } else {
            if (isConv) {
                if (F_DELAY) __builtin_amdgcn_s_sleep(F_DELAY);
                wait_flags(rw, W_F1, tlo, thi, lane);
                cset(C_F1);
                STAMP(2);
                __builtin_amdgcn_s_setprio(3);
                bar();
#pragma unroll
                for (int i = 0; i < 16; ++i) st128<SC1>(s4, rw, ALIAS ? (lane >> 2) * US + (lane & 3) * 16 + 16 * US * i + cj * 64 : cj * 16384 + lane * 16 + 1024 * i, W_Y);
                __builtin_amdgcn_s_setprio(0);
                cset(C_W0);
                DRAIN();
                cadd(C_YL);
                if (lane == 0) st64<SC1>(v2u{tlo, thi}, rw, 0, W_F2 + wg * 8);
            } else {
                if (lane == 0) st64<SC1>(v2u{tlo, thi}, rw, 0, W_F2 + wg * 8);
                cwait<4>(C_GO, 0x54u);
                __builtin_amdgcn_s_sleep(32);
            }
            wait_flag(rw, W_F3 + xq * 128, tlo, thi);
            STAMP(3);
            u32 vo[1] = {(u32)(lane & 3) * 16};
            v4u sv[1];
            poll16s<1, 16>(rw, vo, xs, sv);
            if (lane < 4) *(LAS v4u*)L(L_SEL + lane * 16) = sv[0];
            LGKM0();
            cset(C_SEL, 0x100u | selmask(b));
            STAMP(4);
            wait_flags(rw, W_F2, tlo, thi, lane);
            cset(C_F2);
            STAMP(5);
            if (isConv) {
                const int s = cj / 18, cb = cj % 18, d = 32 * cb + (lane & 31);
                const v2u me = *(LAS v2u*)L(L_SEL + s * 8);
                const int tok = (2047 - me.x) & 2047;
                const u32 ord = me.x >> 16, rb = (ord & 0x8000u) ? (ord & 0x7FFFu) : (~ord & 0xFFFFu);
                const float mb = lo16(me.y), inv = __builtin_amdgcn_rcpf(hi16(me.y));
                const u32 eo = (tok * D + d) * 2;
                const rsrc_t rk = mkrs(p.k, S * D * 2);
                const u32 xb = __builtin_amdgcn_raw_buffer_load_b16(rq, eo, 0, 0), vb = __builtin_amdgcn_raw_buffer_load_b16(mkrs(p.v, S * D * 2), eo, 0, 0);
                u32 kb[4];
#pragma unroll
                for (int j = 0; j < 4; ++j) kb[j] = __builtin_amdgcn_raw_buffer_load_b16(rk, tok >= j ? eo - j * 1152 : OOB, 0, 0);
                const v2u sw = ld64<0>(mkrs(p.sk, D * 8), d * 8, 0);
                const u32 db = __builtin_amdgcn_raw_buffer_load_b16(mkrs(p.dv, D * 2), d * 2, 0, 0);
                const float x = lo16(xb);
                const float kv = rbf((lo16(kb[3]) * hi16(sw.y) + lo16(kb[2]) * lo16(sw.y) + lo16(kb[1]) * hi16(sw.x) + lo16(kb[0]) * lo16(sw.x)) * lo16(vb));
                const float xn = rbf((x - mb) * inv);
                float base = x + xn * kv * lo16(db);
                float rsg = rbf(__builtin_amdgcn_rcpf(1.0f + __expf(-lo16(rb))));
                const int c = (d & 1) * 288 + (d >> 1);
                u32 yo = (ALIAS ? c * 16 + (tok >> 3) * US : (c >> 2) * 16384 + (c & 3) * 16 + (tok >> 3) * 64) + ((tok & 7) >> 1) * 4;
                u32 vp[9];
#pragma unroll
                for (int i = 0; i < 9; ++i) vp[i] = (8 * i + (lane >> 3)) * 18432 + (lane & 7) * 16;
                asm volatile("" : "+v"(base), "+v"(rsg), "+v"(yo));
                cwait<2>(C_YST, 4);
                if (Y_DELAY) __builtin_amdgcn_s_sleep(Y_DELAY);
                if (PH_AT < 2) {
                    wait_flag(rw, W_PH + (cj % 72) * 128, tlo, thi);
                    if (P_DELAY) __builtin_amdgcn_s_sleep(P_DELAY);
                } else {
                    wait_word<2>(rw, W_PART + (cj % 72) * 18432 + s * 2304 + cb * 128);
                }
                STAMP(6);
                v4u pv[9];
                u32 yw = ld32<SC1>(rw, yo, W_Y);
                poll16<9>(rw, vp, W_PART + s * 2304 + cb * 128, pv);
                STAMP(7);
                f4 a = {0.f, 0.f, 0.f, 0.f};
#pragma unroll
                for (int i = 0; i < 9; ++i) a += __builtin_bit_cast(f4, pv[i]);
#pragma unroll
                for (int i = 0; i < 4; ++i) {
                    float t = a[i];
                    t += asf(dpp<0x128>(asu(t)));
                    const auto r1 = __builtin_amdgcn_permlane16_swap(asu(t), asu(t), false, false);
                    t = asf(r1[0]) + asf(r1[1]);
                    const auto r2 = __builtin_amdgcn_permlane32_swap(asu(t), asu(t), false, false);
                    a[i] = asf(r2[0]) + asf(r2[1]);
                }
                if (lane < 8) *(LAS f4*)L(L_RED + lane * 16) = a;
                LGKM0();
                base += rbf(xn + rbf(rbf(*(LAS float*)L(L_RED + (lane & 31) * 4)) * rsg));
                asm volatile("" : "+v"(base));
                STAMP(8);
                NOUNROLL
                for (u32 n = 0; n < SPIN_CAP; ++n) {
                    if (__all(yw != SENT)) break;
                    FENCE();
                    yw = ld32<SC1>(rw, yo, W_Y);
                }
                const float o = base + xn * ((tok & 1) ? hi16(yw) : lo16(yw));
                if (lane < 32) __builtin_amdgcn_raw_buffer_store_b16((u16)pk(o, 0.f), mkrs(p.out, S * D * 2), eo, 0, AUX_OUT);
                STAMP(9);
            }
        }
    } else {
        if (isK) {
            u32 uo = cp < 288 ? W_U + b * US + cp * 16 : OOB;
            asm volatile("" : "+v"(uo));
            const f2 w0 = {lo16(sk.x), lo16(sk.z)}, w1 = {hi16(sk.x), hi16(sk.z)}, w2 = {lo16(sk.y), lo16(sk.w)}, w3 = {hi16(sk.y), hi16(sk.w)};
            f2 kf[11], r[8];
#pragma unroll
            for (int i = 0; i < 11; ++i) kf[i] = unpk(ra[i]);
#pragma unroll
            for (int t = 0; t < 8; ++t) {
                r[t] = kf[t] * w3 + kf[t + 1] * w2 + kf[t + 2] * w1 + kf[t + 3] * w0;
                asm volatile("" : "+v"(r[t]));
            }
            asm volatile("" ::"v"(rb[7]));
            STAMP(10);
            SB();
            v4u hd[4];
            if (isConv && mw < 4) {
                const int c = 4 * cj + mw;
                const rsrc_t rh = mkrs(p.h, D * S * 2);
#pragma unroll
                for (int i = 0; i < 4; ++i) hd[i] = ld128<AUX_IN>(rh, lane * 16 + 1024 * i, (c < 288 ? 2 * c : 2 * (c - 288) + 1) * 4096);
            }
            SB();
#pragma unroll
            for (int t = 0; t < 8; ++t) r[t] *= unpk(rb[t]);
            const u32 clr = cpeek(C_CLR);
            v4u c0 = v4u{nos(pk(r[7].x, r[6].x)), nos(pk(r[5].x, r[4].x)), nos(pk(r[3].x, r[2].x)), nos(pk(r[1].x, r[0].x))};
            v4u c1 = v4u{nos(pk(r[7].y, r[6].y)), nos(pk(r[5].y, r[4].y)), nos(pk(r[3].y, r[2].y)), nos(pk(r[1].y, r[0].y))};
            asm volatile("" : "+v"(c0), "+v"(c1));
            STAMP(1);
            if (!__builtin_amdgcn_readfirstlane(clr)) cwait(C_CLR);
            st128<SC1>(c0, rw, uo, 0);
            st128<SC1>(c1, rw, uo + 4608, 0);
            cadd(C_GO, 0x10u);
            STAMP(2);
            u32 qq[8], dvp;
            {
                const u32 vo = cp < 288 ? cp * 4 : OOB;
#pragma unroll
                for (int i = 0; i < 8; ++i) qq[i] = ld32<0>(rq, vo, (8 * b + i) * 1152);
                dvp = ld32<0>(mkrs(p.dv, D * 2), vo, 0);
            }
            if (isConv && mw < 4) {
                LAS char* hb = L(L_H + mw * HCH);
#pragma unroll
            for (int i = 0; i < 4; ++i) {
                const v4u dd = hd[i];
                const u32 e0 = i < 3 ? __builtin_amdgcn_readfirstlane(hd[i < 3 ? i + 1 : 3].x) : 0u;
                const u32 e1 = i < 3 ? __builtin_amdgcn_readfirstlane(hd[i < 3 ? i + 1 : 3].y) : 0u;
                const u32 n0 = __builtin_amdgcn_update_dpp(e0, dd.x, 0x130, 0xF, 0xF, false);
                const u32 n1 = __builtin_amdgcn_update_dpp(e1, dd.y, 0x130, 0xF, 0xF, false);
                const u32 a01 = __builtin_amdgcn_alignbit(dd.y, dd.x, 16), a12 = __builtin_amdgcn_alignbit(dd.z, dd.y, 16),
                          a23 = __builtin_amdgcn_alignbit(dd.w, dd.z, 16), a3n = __builtin_amdgcn_alignbit(n0, dd.w, 16),
                          ann = __builtin_amdgcn_alignbit(n1, n0, 16);
                LAS char* o = hb + (lane + 64 * i + 2) * 16;
                *(LAS v4u*)(o) = dd;
                *(LAS v4u*)(o + HC) = v4u{a01, a12, a23, a3n};
                *(LAS v4u*)(o + 2 * HC) = v4u{dd.y, dd.z, dd.w, n0};
                *(LAS v4u*)(o + 3 * HC) = v4u{a12, a23, a3n, ann};
            }
            {
                const u32 h0 = __builtin_amdgcn_readfirstlane(hd[0].x), h1 = __builtin_amdgcn_readfirstlane(hd[0].y);
                if (lane < 8) {
                    const bool od = lane & 1;
                    const int sg = lane >> 1;
                    v4u z = {0u, 0u, 0u, 0u};
                    z.z = od && sg == 3 ? h0 << 16 : 0u;
                    z.w = !od || sg == 0 ? 0u : (sg == 1 ? h0 << 16 : (sg == 2 ? h0 : (h0 >> 16) | (h1 << 16)));
                    *(LAS v4u*)(hb + sg * HC + (lane & 1) * 16) = z;
                }
            }
                STAMP(7);
            }
            {
                const f2 dv = unpk(dvp);
                cwait_eq(C_GO, 0xFu, 4u);
                u32 xp[8];
#pragma unroll
                for (int t = 0; t < 8; ++t) {
                    const f2 st = *(LAS f2*)L(L_STAT + t * 8);
                    const f2 x = unpk(qq[t]);
                    xp[t] = pk2((x - st.x) * st.y);
                    const f2 xn = unpk(xp[t]);
                    const u32 a0 = c0[(7 - t) >> 1], a1 = c1[(7 - t) >> 1];
                    const f2 kv = (t & 1) ? f2{lo16(a0), lo16(a1)} : f2{hi16(a0), hi16(a1)};
                    const f2 pb = x + xn + xn * (kv * dv);
                    *(LAS f2*)L(L_ST + 10240 + t * 2560 + cp * 8) = pb;
                }
                *(LAS v4u*)L(L_ST + cp * 16) = v4u{xp[0], xp[1], xp[2], xp[3]};
                *(LAS v4u*)L(L_ST + 5120 + cp * 16) = v4u{xp[4], xp[5], xp[6], xp[7]};
                STAMP(11);
            }
            if (isSel) {
                if (cp < 48) *(LAS v4u*)L(L_LK + cp * 16) = v4u{0u, 0u, 0u, 0u};
            }
        } else if (isConv) {
            LAS char* hb = L(L_H + lw * HCH);
            const int l = tid - 384;
            if (lane < 32) *(LAS v4u*)L(L_U + lw * UCH + lane * 16) = v4u{0u, 0u, 0u, 0u};
            f4 acc[8];
#pragma unroll
            for (int i = 0; i < 8; ++i) {
                acc[i] = f4{0.f, 0.f, 0.f, 0.f};
                asm volatile("" : "+v"(acc[i]));
            }
            u32 vo[4];
#pragma unroll
            for (int i = 0; i < 4; ++i) vo[i] = (64 * i + (l >> 2)) * US + (l & 3) * 16;
            const int m1 = (lane & 15) + 1, kq = lane >> 4;
            u32 va = (u32)(uintptr_t)(hb + (m1 & 3) * HC + 16 * kq + 8 * (m1 >> 2));
            u32 vb = (u32)(uintptr_t)L(L_U + lw * UCH + (33 + 2 * (lane & 15) - kq) * 16 - 448);
            u32 uw = (u32)(uintptr_t)L(L_U + (l & 3) * UCH + (32 + (l >> 2)) * 16);
            u32 yw = (u32)(uintptr_t)L(L_Y + (32 * (kq & 1) + 2 * (lane & 15) + (kq >> 1)) * 64 + ((lw ^ lane) & 3) * 16);
            u32 yr = (u32)(uintptr_t)L(L_Y + (l >> 2) * 64 + ((l ^ (l >> 3)) & 3) * 16);
            u32 yg = W_Y + (ALIAS ? cj * 64 + (l >> 2) * US + (l & 3) * 16 : cj * 16384 + l * 16);
            asm volatile("" : "+v"(vo[0]), "+v"(vo[1]), "+v"(vo[2]), "+v"(vo[3]), "+v"(va), "+v"(vb), "+v"(uw), "+v"(yw), "+v"(yr), "+v"(yg));
            SB();
            STAMP(3);
            ln_part(rw, b, lw, lane, ra, rb, ts);
            cwait_eq(C_GO, 0xF0u, 0x50u);
            if (U_DELAY) __builtin_amdgcn_s_sleep(U_DELAY);
            cwait(C_F1);
            STAMP(4);
            v4u uv[4];
            poll_issue<4>(rw, vo, W_U + cj * 64, uv);
            poll_finish<4>(rw, vo, W_U + cj * 64, uv);
#pragma unroll
            for (int i = 0; i < 4; ++i) *(LAS v4u*)((LAS char*)(uintptr_t)uw + 1024 * i) = uv[i];
            STAMP(5);
            bar();
            STAMP(8);
            AF a0[4], a1[4];
            v4u b0[4], b1[4];
#pragma unroll
            for (int n = 0; n < 4; ++n) {
                DSR128(b0[n], vb, 448 + 512 * n);
                DSR64(a0[n].lo, va, 512 * n);
                DSR64(a0[n].hi, va, 512 * n + 8);
            }
            conv_phase<0>(va, vb, acc, a0, b0, a1, b1);
            conv_phase<1>(va, vb, acc, a1, b1, a0, b0);
            conv_phase<2>(va, vb, acc, a0, b0, a1, b1);
            conv_phase<3>(va, vb, acc, a1, b1, a0, b0);
            conv_phase<4>(va, vb, acc, a0, b0, a1, b1);
            conv_phase<5>(va, vb, acc, a1, b1, a0, b0);
            conv_phase<6>(va, vb, acc, a0, b0, a1, b1);
            conv_phase<7>(va, vb, acc, a1, b1, a0, b0);
            asm volatile("s_nop 7\n\ts_nop 1" : "+v"(acc[0]), "+v"(acc[1]), "+v"(acc[2]), "+v"(acc[3]), "+v"(acc[4]), "+v"(acc[5]), "+v"(acc[6]), "+v"(acc[7]));
            STAMP(6);
#pragma unroll
            for (int q = 0; q < 8; q += 2) {
                const u32 y0 = nos(pk(acc[q][0], acc[q][1])), y1 = nos(pk(acc[q][2], acc[q][3]));
                const u32 z0 = nos(pk(acc[q + 1][0], acc[q + 1][1])), z1 = nos(pk(acc[q + 1][2], acc[q + 1][3]));
                const auto s0 = __builtin_amdgcn_permlane16_swap(y0, z0, false, false);
                const auto s1 = __builtin_amdgcn_permlane16_swap(y1, z1, false, false);
                *(LAS v4u*)(uintptr_t)(yw + q * 2048) = v4u{s0[0], s1[0], s0[1], s1[1]};
            }
            cadd(C_YL);
            cwait<0>(C_YL, 5);
            {
                v4u yv[4];
#pragma unroll
                for (int i = 0; i < 4; ++i) yv[i] = *(LAS v4u*)(uintptr_t)(yr + 4096 * i);
#pragma unroll
                for (int i = 0; i < 4; ++i) st128<SC1>(yv[i], rw, yg + (ALIAS ? 64 * US : 4096) * i, 0);
            }
            cadd(C_YST);
            STAMP(7);
        } else {
            ln_part(rw, b, lw, lane, ra, rb, ts);
        }

        if (isMoe) {
            const int c = lane & 15, kq = lane >> 4;
            const GAS char* gp = (const GAS char*)(c < 8 ? p.wg : p.wu) + (size_t)(64 * mw + 8 * kq) * (F * 2) + mu * 128 + (c & 7) * 16;
            u32 xo = (64 * mw + 8 * kq) * 2;
            u32 ro = (u32)(uintptr_t)L(L_R + ((mw * 8 + 4 * (kq & 1)) * 128 + 8 * c) * 4);
            u32 po = W_PART + mu * 18432 + mw * 256 + (c & 7) * 2304 + (c >> 3) * 64 + kq * 16;
            u32 hwo = (u32)(uintptr_t)L(L_HA + mw * HAS + ((lane & 32) + 8 * ((lane & 15) >> 2) + 4 * ((lane >> 4) & 1) + (lane & 3)) * 2);
            u32 hro = (u32)(uintptr_t)L(L_HA + (c & 7) * HAS + 16 * kq), rro = (u32)(uintptr_t)L(L_R + (mw * 128 + lane) * 4);
            u32 ta[4];
#pragma unroll
            for (int i = 0; i < 4; ++i) {
                const int row = 16 * i + 4 * kq + (c >> 2);
                ta[i] = (u32)(uintptr_t)L(mw < 8 ? L_DN + row * DNS + 128 * mw + 8 * (c & 3) : L_DT + row * 128 + 8 * (c & 3));
                asm volatile("" : "+v"(ta[i]));
            }
            asm volatile("" : "+v"(xo), "+v"(ro), "+v"(po), "+v"(hwo), "+v"(hro), "+v"(rro));
            v4u gr[8], gf[2][8], xa0, xa1;
            v2u me;
            v2u dl[2][4], dh[2][4];
            cwait<2>(C_WI);
            STAMP(11);
#pragma unroll
            for (int ks = 0; ks < 2; ++ks) {
                if (ks == 1 && G1_DELAY) __builtin_amdgcn_s_sleep(G1_DELAY);
#pragma unroll
                for (int e = 0; e < 8; ++e) gr[e] = *(const GAS v4u*)(gp + (size_t)(32 * ks + e) * (F * 2));
                SB();
                if (ks == 1) {
                    cwait(C_XS);
                    me = *(LAS v2u*)L(L_SEL + (c & 7) * 8);
                    const u32 ro2 = ((2047 - me.x) & 2047) * 1152 + xo;
                    xa0 = ld128<0>(rq, ro2, 0);
                    xa1 = ld128<0>(rq, ro2 + 64, 0);
                    SB();
                }
#pragma unroll
                for (int nn = 0; nn < 8; ++nn)
#pragma unroll
                    for (int pp = 0; pp < 4; ++pp)
                        gf[ks][nn][pp] = __builtin_amdgcn_perm(gr[2 * pp + 1][nn >> 1], gr[2 * pp][nn >> 1], (nn & 1) ? 0x07060302u : 0x05040100u);
#pragma unroll
                for (int nn = 0; nn < 8; ++nn) asm volatile("" : "+v"(gf[ks][nn]));
                SB();
                if (ks == 0) STAMP(7);
            }
            STAMP(8);
            {
                const float mb = lo16(me.y), inv = __builtin_amdgcn_rcpf(hi16(me.y));
#pragma unroll
                for (int i = 0; i < 4; ++i) {
                    xa0[i] = pk2((unpk(xa0[i]) - mb) * inv);
                    xa1[i] = pk2((unpk(xa1[i]) - mb) * inv);
                }
                STAMP(5);
                f4 g[8];
#pragma unroll
                for (int nn = 0; nn < 8; ++nn) g[nn] = mfma(xa1, gf[1][nn], mfma(xa0, gf[0][nn], f4{0.f, 0.f, 0.f, 0.f}));
#pragma unroll
                for (int r = 0; r < 4; ++r) {
                    const f4 o = kq < 2 ? f4{g[0][r], g[1][r], g[2][r], g[3][r]} : f4{g[4][r], g[5][r], g[6][r], g[7][r]};
                    *(LAS f4*)(uintptr_t)(ro + r * 512 + (kq >> 1) * 16) = o;
                }
            }
            bar();
            if (mw < 8) {
                float gv[9], uv[9];
#pragma unroll
                for (int w = 0; w < 9; ++w) {
                    gv[w] = *(LAS float*)(uintptr_t)(rro + w * 4096);
                    uv[w] = *(LAS float*)(uintptr_t)(rro + w * 4096 + 256);
                }
                SB();
                const float g = rbf(((gv[0] + gv[1]) + (gv[2] + gv[3])) + ((gv[4] + gv[5]) + (gv[6] + gv[7])) + gv[8]);
                const float u = rbf(((uv[0] + uv[1]) + (uv[2] + uv[3])) + ((uv[4] + uv[5]) + (uv[6] + uv[7])) + uv[8]);
                const float sg = rbf(g * __builtin_amdgcn_rcpf(1.0f + __expf(-g)));
                *(LAS u16*)(uintptr_t)hwo = (u16)pk(sg * u, 0.f);
            }
            bar();
            STAMP(9);
            {
                v4u ha0, ha1;
                DSR128(ha0, hro, 0);
                DSR128(ha1, hro, 64);
#pragma unroll
                for (int h = 0; h < 2; ++h)
#pragma unroll
                    for (int nt = 0; nt < 4; ++nt) {
                        asm volatile("ds_read_b64_tr_b16 %0, %1 offset:%2" : "=v"(dl[h][nt]) : "v"(ta[2 * h]), "i"(32 * nt));
                        asm volatile("ds_read_b64_tr_b16 %0, %1 offset:%2" : "=v"(dh[h][nt]) : "v"(ta[2 * h + 1]), "i"(32 * nt));
                    }
                asm volatile("s_waitcnt lgkmcnt(0)" : "+v"(ha0), "+v"(ha1), "+v"(dl[0][0]), "+v"(dl[0][1]), "+v"(dl[0][2]), "+v"(dl[0][3]), "+v"(dl[1][0]),
                             "+v"(dl[1][1]), "+v"(dl[1][2]), "+v"(dl[1][3]), "+v"(dh[0][0]), "+v"(dh[0][1]), "+v"(dh[0][2]), "+v"(dh[0][3]),
                             "+v"(dh[1][0]), "+v"(dh[1][1]), "+v"(dh[1][2]), "+v"(dh[1][3]));
                SB();
                f4 y[4];
#pragma unroll
                for (int nt = 0; nt < 4; ++nt)
                    y[nt] = mfma(v4u{dl[1][nt].x, dl[1][nt].y, dh[1][nt].x, dh[1][nt].y}, ha1,
                                 mfma(v4u{dl[0][nt].x, dl[0][nt].y, dh[0][nt].x, dh[0][nt].y}, ha0, f4{0.f, 0.f, 0.f, 0.f}));
#pragma unroll
                for (int i = 0; i < 2; ++i) {
                    const f4 o = c < 8 ? y[2 * i] : y[2 * i + 1];
                    st128<SC1>(v4u{nos(asu(o[0])), nos(asu(o[1])), nos(asu(o[2])), nos(asu(o[3]))}, rw, po + 128 * i, 0);
                }
            }
            STAMP(10);
        } else if (isSel) {
            const int sw = mw < 4 ? mw : mw - 1;
            u32 vo[2] = {(u32)(sw * 64 + lane) * 16, (u32)(sw * 64 + lane) * 16 + 8192};
            v4u kv2[2];
            asm volatile("" : "+v"(vo[0]), "+v"(vo[1]));
            if (!isK && K_DELAY) __builtin_amdgcn_s_sleep(K_DELAY);
            bar();
            if (mw != 4) {
                poll16<2>(rw, vo, W_KEY, kv2);
                u32 mx = max32(umax(umax(kv2[0].x, kv2[0].z), umax(kv2[1].x, kv2[1].z)));
                const auto s2 = __builtin_amdgcn_permlane32_swap(mx, mx, false, false);
                if (lane == 0) *(LAS u32*)L(L_WM + 4 * sw) = umax(s2[0], s2[1]);
            }
            STAMP(7);
            bar();
            if (mw != 4) {
                const v4u m0 = *(LAS v4u*)L(L_WM), m1 = *(LAS v4u*)L(L_WM + 16);
                const u32 thr = umin(umin(umin(m0.x, m0.y), umin(m0.z, m0.w)), umin(umin(m1.x, m1.y), umin(m1.z, m1.w)));
                u32 key[4] = {kv2[0].x, kv2[0].z, kv2[1].x, kv2[1].z};
                const u32 ms[4] = {kv2[0].y, kv2[0].w, kv2[1].y, kv2[1].w};
                u64 bm[4];
                u32 tot = 0;
#pragma unroll
                for (int i = 0; i < 4; ++i) {
                    bm[i] = __builtin_amdgcn_ballot_w64(key[i] >= thr);
                    tot += __builtin_popcountll(bm[i]);
                }
                if (tot <= 8u) {
                    u32 pos = 8 * sw;
#pragma unroll
                    for (int i = 0; i < 4; ++i) {
                        const u32 at = __builtin_amdgcn_mbcnt_hi((u32)(bm[i] >> 32), __builtin_amdgcn_mbcnt_lo((u32)bm[i], pos));
                        *(LAS v2u*)L(L_LK + (key[i] >= thr ? at * 8 : 1024u)) = v2u{key[i], ms[i]};
                        pos += __builtin_popcountll(bm[i]);
                    }
                } else {
                    NOUNROLL
                    for (int r = 0; r < 8; ++r) {
                        u32 mx = max32(umax(umax(key[0], key[1]), umax(key[2], key[3])));
                        const auto s2 = __builtin_amdgcn_permlane32_swap(mx, mx, false, false);
                        mx = umax(s2[0], s2[1]);
#pragma unroll
                        for (int i = 0; i < 4; ++i)
                            if (key[i] == mx) {
                                *(LAS v2u*)L(L_LK + (8 * sw + r) * 8) = v2u{key[i], ms[i]};
                                key[i] = 0u;
                            }
                    }
                }
            }
            bar();
            const v2u mine = *(LAS v2u*)L(L_LK + lane * 8);
            if (mw != 4) {
                u32 cnt = 0;
#pragma unroll
                for (int u = 0; u < 4; ++u) {
                    const v4u t = *(LAS v4u*)L(L_LK + sw * 64 + u * 16);
                    cnt += (t.x > mine.x) + (t.z > mine.x);
                }
                __hip_atomic_fetch_add((LAS u32*)L(L_RK + lane * 4), cnt, __ATOMIC_RELAXED, __HIP_MEMORY_SCOPE_WORKGROUP);
            }
            bar();
            STAMP(9);
            {
                const u32 rank = *(LAS u32*)L(L_RK + lane * 4);
                if (mw == 0) {
                    st64<SC1>(mine, rw, rank < 8u ? rank * 8 : OOB, xs);
                    if (rank < 8u) *(LAS v2u*)L(L_SEL + rank * 8) = mine;
                    LGKM0();
                    cset(C_XS);
                }
            }
            STAMP(10);
        }

        if (isK) {
            const u32 vo = cp < 288 ? cp * 4 : OOB;
            f2 xn[8], pb[8];
            {
                const v4u xa = *(LAS v4u*)L(L_ST + cp * 16), xb = *(LAS v4u*)L(L_ST + 5120 + cp * 16);
#pragma unroll
                for (int t = 0; t < 8; ++t) {
                    pb[t] = *(LAS f2*)L(L_ST + 10240 + t * 2560 + cp * 8);
                    xn[t] = unpk(t < 4 ? xa[t] : xb[t - 4]);
                }
            }
            u32 vy[2] = {ALIAS ? (u32)cp * 16 : (u32)((cp >> 2) * 16384 + (cp & 3) * 16), ALIAS ? (u32)cp * 16 + 4608 : (u32)(((cp >> 2) + 72) * 16384 + (cp & 3) * 16)};
#pragma unroll
            for (int t = 0; t < 8; ++t) asm volatile("" : "+v"(xn[t]), "+v"(pb[t]));
            asm volatile("" : "+v"(vy[0]), "+v"(vy[1]));
            if (isConv) {
                bar();
            }
            const u32 mask = cwait<4>(C_SEL);
            u32 sr[8];
#pragma unroll
            for (int t = 0; t < 8; ++t) sr[t] = ((mask >> t) & 1u) ? OOB : (8 * b + t) * 1152;
            cwait<4>(C_F2);
            STAMP(3);
            if (isConv) {
                cwait<4>(C_YST, 4);
                if (Y_DELAY) __builtin_amdgcn_s_sleep(Y_DELAY);
            } else if (!isMoe || M_HINT) {
                wait_word<4>(rw, W_Y + (ALIAS ? (18 * xq + 17) * 64 + b * US : (18 * xq + 17) * 16384 + b * 64));
            }
            STAMP(4);
            if (cp < 288) {
                v4u yv[2];
                poll16<2>(rw, vy, W_Y + b * (ALIAS ? US : 64), yv);
                STAMP(5);
                const rsrc_t ro = mkrs(p.out, S * D * 2);
#pragma unroll
                for (int t = 0; t < 8; ++t) {
                    const u32 c0 = yv[0][t >> 1], c1 = yv[1][t >> 1];
                    const f2 y = (t & 1) ? f2{hi16(c0), hi16(c1)} : f2{lo16(c0), lo16(c1)};
                    st32<AUX_OUT>(pk2(pb[t] + xn[t] * y), ro, vo, sr[t]);
                }
                STAMP(6);
            }
        }
    }
}

extern "C" void kernel_launch(void* const* d_in, const int* in_sizes, int n_in, void* d_out, int out_size, void* d_ws, size_t ws_size,
                              hipStream_t stream) {
    Params p;
    p.q = (const u16*)d_in[0];
    p.k = (const u16*)d_in[1];
    p.v = (const u16*)d_in[2];
    p.sk = (const u16*)d_in[3];
    p.h = (const u16*)d_in[4];
    p.dv = (const u16*)d_in[5];
    p.wr = (const u16*)d_in[7];
    p.wg = (const u16*)d_in[8];
    p.wu = (const u16*)d_in[9];
    p.wd = (const u16*)d_in[10];
    p.out = (u16*)d_out;
    p.ws = (char*)d_ws + (WS_AT_END ? ((ws_size - WS_BYTES) & ~(size_t)4095) : 0);
    fused_kernel<<<256, NT, 0, stream>>>(p);
}
```
